```python
import torch

def _softmax(x, axis=-1):
    return torch.softmax(x, dim=axis)
def _gelu(x):
    return torch.nn.functional.gelu(x, approximate="tanh")
def _silu(x):
    return torch.nn.functional.silu(x)
def _sigmoid(x):
    return torch.sigmoid(x)
def _layernorm(x, axis=-1, eps=1e-5):
    m = x.mean(dim=axis, keepdim=True)
    v = x.var(dim=axis, keepdim=True, unbiased=False)
    return (x - m) / torch.sqrt(v + eps)
def _scatter_add(tgt, idx, src, axis=0):
    return tgt.clone().index_add_(axis, idx, src)
def _segment_sum(src, seg, n):
    out = torch.zeros((n,) + src.shape[1:], dtype=src.dtype)
    return out.index_add_(0, seg, src)
def _pad(x, pw):
    flat = tuple(v for pair in reversed(pw) for v in pair)
    return torch.nn.functional.pad(x, flat)

kernel_name = 'gated_mix__gqa_block_cached__glu_gated_glu__decode'

def setup_inputs():

    _w = (lambda a: (lambda t: t.to(torch.bfloat16) if t.is_floating_point() else t)(torch.as_tensor(a)))
    def _A_setup():
        B, Sq, D = 4, 1, 512
        Hq, Hkv, Sk, Dh = 32, 32, 4096, 128
        assert Hq % Hkv == 0, (Hq, Hkv)
        return {
            "x": _w((lambda *s: torch.randn(*s, dtype=torch.bfloat16) * 0.05)(B, Sq, D)),
            "k_cache": _w((lambda *s: torch.randn(*s, dtype=torch.bfloat16) * 6.4)(B, Hkv, Sk, Dh)),
            "v_cache": _w((lambda *s: torch.randn(*s, dtype=torch.bfloat16) * 0.4)(B, Hkv, Sk, Dh)),
            "wq": _w((lambda *s: torch.randn(*s, dtype=torch.bfloat16) * 6.4)(D, Hq * Dh) * (D ** -0.5)),
            "wo": _w((lambda *s: torch.randn(*s, dtype=torch.bfloat16) * 0.4)(Hq * Dh, D) * ((Hq * Dh) ** -0.5)),
        }
    _A_in = _A_setup()

    def _B_setup():
        B, S, D, F = 4, 1, 512, 1376
        return {
            "x": _w((lambda *s: torch.randn(*s, dtype=torch.bfloat16) * 0.05)(B, S, D)),
            "w_gate": _w((lambda *s: torch.randn(*s, dtype=torch.bfloat16) * 0.4)(D, F)),
            "w_up": _w((lambda *s: torch.randn(*s, dtype=torch.bfloat16) * 0.05)(D, F)),
            "w_down": _w((lambda *s: torch.randn(*s, dtype=torch.bfloat16) * 0.05)(F, D)),
        }
    _B_in = _B_setup()

    return {
        "A_x": _A_in["x"],
        "A_k_cache": _A_in["k_cache"],
        "A_v_cache": _A_in["v_cache"],
        "A_wq": _A_in["wq"],
        "A_wo": _A_in["wo"],
        "B_x": _B_in["x"],
        "B_w_gate": _B_in["w_gate"],
        "B_w_up": _B_in["w_up"],
        "B_w_down": _B_in["w_down"],
        "gate_w": _w((lambda *s: torch.randn(*s, dtype=torch.bfloat16) * 0.05)(512) * 16.0),
    }

def reference(A_x, A_k_cache, A_v_cache, A_wq, A_wo, B_x, B_w_gate, B_w_up, B_w_down, gate_w):
    def _A_ref(x, k_cache, v_cache, wq, wo):
        B, Sq, D = x.shape
        Hq, Dh = 32, 128
        Hkv = k_cache.shape[1]
        rep = Hq // Hkv
        q = (lambda x, *d: x.permute(*d))((x @ wq).reshape(B, Sq, Hq, Dh), 0, 2, 1, 3)
        k = (lambda x, n, axis: torch.repeat_interleave(x, n, dim=axis))(k_cache, rep, axis=1)
        v = (lambda x, n, axis: torch.repeat_interleave(x, n, dim=axis))(v_cache, rep, axis=1)
        scores = torch.einsum("bhqd,bhkd->bhqk", q, k) * (Dh ** -0.5)
        p = _softmax(scores, axis=-1)
        o = torch.einsum("bhqk,bhkd->bhqd", p, v)
        return (lambda x, *d: x.permute(*d))(o, 0, 2, 1, 3).reshape(B, Sq, Hq * Dh) @ wo

    def _B_ref(x, w_gate, w_up, w_down):
        g = torch.einsum("bsd,df->bsf", x, w_gate)
        u = torch.einsum("bsd,df->bsf", x, w_up)
        h = _sigmoid(g) * u
        return torch.einsum("bsf,fd->bsd", h, w_down)

    x = A_x
    g = _sigmoid((x * gate_w).sum(-1, keepdims=True))
    a = _A_ref(x, A_k_cache, A_v_cache, A_wq, A_wo)
    b = _B_ref(x, B_w_gate, B_w_up, B_w_down)
    return g * a + (1 - g) * b
```

```cpp
#include <hip/hip_runtime.h>
#include <hip/hip_bf16.h>
#include <cstdint>

#define MIMIC 1

namespace {
typedef unsigned v4u __attribute__((ext_vector_type(4)));
typedef unsigned v2u __attribute__((ext_vector_type(2)));
typedef float f32x4 __attribute__((ext_vector_type(4)));
typedef __bf16 bf16x2 __attribute__((ext_vector_type(2)));

constexpr int NB = 4, DM = 512, HQ = 32, SK = 4096, DH = 128, FF = 1376;
constexpr int NWG = 256, NTHR = 512, NW = 8;
constexpr int NSL = 43;
constexpr int RSW3 = 0x00020000;
constexpr int AUX_NT = 2;
constexpr float LOG2E = 1.4426950408889634f;
constexpr float SCALE = 0.08838834764831845f;

constexpr size_t WS_Y = 0;
constexpr size_t WS_ML = WS_Y + 256 * 512;
constexpr size_t WS_YM = WS_ML + 512;

__device__ __forceinline__ float dot2(unsigned a, unsigned b, float acc) {
  return __builtin_amdgcn_fdot2_f32_bf16(__builtin_bit_cast(bf16x2, a), __builtin_bit_cast(bf16x2, b), acc, false);
}
__device__ __forceinline__ float bflo(unsigned u) { return __builtin_bit_cast(float, u << 16); }
__device__ __forceinline__ float bfhi(unsigned u) { return __builtin_bit_cast(float, u & 0xffff0000u); }
__device__ __forceinline__ unsigned pk_bf16(float lo, float hi) {
  bf16x2 r; r.x = (__bf16)lo; r.y = (__bf16)hi; return __builtin_bit_cast(unsigned, r);
}
__device__ __forceinline__ float rbf(float x) { return (float)(__bf16)x; }
template <int CTRL> __device__ __forceinline__ float dpp(float x) {
  return __builtin_bit_cast(float, __builtin_amdgcn_mov_dpp(__builtin_bit_cast(int, x), CTRL, 0xf, 0xf, true));
}
constexpr int XOR1 = 0xB1, XOR2 = 0x4E, XOR7 = 0x141, XOR8 = 0x128, ROR4 = 0x124;
__device__ __forceinline__ float xrow_sum(float x) {
  auto s = __builtin_amdgcn_permlane16_swap(__float_as_uint(x), __float_as_uint(x), false, false);
  x = __uint_as_float(s[0]) + __uint_as_float(s[1]);
  auto t = __builtin_amdgcn_permlane32_swap(__float_as_uint(x), __float_as_uint(x), false, false);
  return __uint_as_float(t[0]) + __uint_as_float(t[1]);
}
__device__ __forceinline__ float xrow_max(float x) {
  auto s = __builtin_amdgcn_permlane16_swap(__float_as_uint(x), __float_as_uint(x), false, false);
  x = fmaxf(__uint_as_float(s[0]), __uint_as_float(s[1]));
  auto t = __builtin_amdgcn_permlane32_swap(__float_as_uint(x), __float_as_uint(x), false, false);
  return fmaxf(__uint_as_float(t[0]), __uint_as_float(t[1]));
}
__device__ __forceinline__ float row_sum(float x) { x += dpp<XOR1>(x); x += dpp<XOR2>(x); x += dpp<XOR7>(x); x += dpp<XOR8>(x); return x; }
__device__ __forceinline__ float row_max(float x) { x = fmaxf(x, dpp<XOR1>(x)); x = fmaxf(x, dpp<XOR2>(x)); x = fmaxf(x, dpp<XOR7>(x)); x = fmaxf(x, dpp<XOR8>(x)); return x; }
__device__ __forceinline__ float sigmoidf_(float x) { return 1.0f / (1.0f + __expf(-x)); }
#define SB() __builtin_amdgcn_sched_barrier(0)

constexpr int L_QP = 0;
constexpr int L_GU = L_QP + 8 * 128;
constexpr int L_P = L_GU + 8 * 64;
constexpr int L_ML = L_P + 8 * 256;
constexpr int L_OP = L_ML + 16;
constexpr int L_YP = L_OP + 8 * 128;
constexpr int L_END = L_YP + 8 * 512;

__global__ void __launch_bounds__(NTHR, 2)
k_main(const unsigned short* __restrict__ x, const unsigned short* __restrict__ kc, const unsigned short* __restrict__ vc,
       const unsigned short* __restrict__ wq, const unsigned short* __restrict__ wo, const unsigned short* __restrict__ wg,
       const unsigned short* __restrict__ wu, const unsigned short* __restrict__ wd, float* __restrict__ ws) {
  __shared__ __attribute__((aligned(16))) float lds[L_END];
  const int tid = threadIdx.x, lane = tid & 63, w = __builtin_amdgcn_readfirstlane(tid >> 6), r = lane >> 4, c = lane & 15;
  const int bid = blockIdx.x, xg = bid & 7, j = bid >> 3;
  const int h = xg * 4 + (j >> 3), b = (j >> 1) & 3, s = j & 1;
  const int slice = xg + 8 * ((j >> 3) * 2 + s);
  const bool has_mlp = slice < NSL;

  const __amdgpu_buffer_rsrc_t rk = __builtin_amdgcn_make_buffer_rsrc((void*)kc, 0, NB * HQ * SK * DH * 2, RSW3);
  const __amdgpu_buffer_rsrc_t rv = __builtin_amdgcn_make_buffer_rsrc((void*)vc, 0, NB * HQ * SK * DH * 2, RSW3);
  const __amdgpu_buffer_rsrc_t rq = __builtin_amdgcn_make_buffer_rsrc((void*)wq, 0, DM * HQ * DH * 2, RSW3);
  const __amdgpu_buffer_rsrc_t ro = __builtin_amdgcn_make_buffer_rsrc((void*)wo, 0, DM * HQ * DH * 2, RSW3);
  const __amdgpu_buffer_rsrc_t rx = __builtin_amdgcn_make_buffer_rsrc((void*)x, 0, NB * DM * 2, RSW3);
  const int mlp_bytes = has_mlp ? DM * FF * 2 : 0;
  const __amdgpu_buffer_rsrc_t rg = __builtin_amdgcn_make_buffer_rsrc((void*)wg, 0, mlp_bytes, RSW3);
  const __amdgpu_buffer_rsrc_t ru = __builtin_amdgcn_make_buffer_rsrc((void*)wu, 0, mlp_bytes, RSW3);
  const __amdgpu_buffer_rsrc_t rd = __builtin_amdgcn_make_buffer_rsrc((void*)wd, 0, mlp_bytes, RSW3);

  const v4u xq0 = __builtin_amdgcn_raw_buffer_load_b128(rx, r * 32, (b * DM + 64 * w) * 2, 0);
  const v4u xq1 = __builtin_amdgcn_raw_buffer_load_b128(rx, r * 32 + 16, (b * DM + 64 * w) * 2, 0);
  const v2u xm = __builtin_amdgcn_raw_buffer_load_b64(rx, (lane >> 2) * 8, (b * DM + 64 * w) * 2, 0);
  SB();
  v4u wt[16];
  {
    const int voff = r * 16 * (HQ * DH * 2) + c * 16, so = 64 * w * (HQ * DH * 2) + h * DH * 2;
#pragma unroll
    for (int i = 0; i < 16; ++i) { wt[i] = __builtin_amdgcn_raw_buffer_load_b128(rq, voff, so + i * (HQ * DH * 2), 0); SB(); }
  }
  v4u mg[4], mu[4], md[4];
  {
    const int voff = (lane >> 2) * 4 * (FF * 2) + (lane & 3) * 16, so = 64 * w * (FF * 2) + slice * 64;
#pragma unroll
    for (int i = 0; i < 4; ++i) { mg[i] = __builtin_amdgcn_raw_buffer_load_b128(rg, voff, so + i * (FF * 2), 0); SB(); }
#pragma unroll
    for (int i = 0; i < 4; ++i) { mu[i] = __builtin_amdgcn_raw_buffer_load_b128(ru, voff, so + i * (FF * 2), 0); SB(); }
    const int voff2 = (lane >> 3) * 4 * (DM * 2) + (lane & 7) * 16, so2 = slice * 32 * (DM * 2) + w * 128;
#pragma unroll
    for (int i = 0; i < 4; ++i) { md[i] = __builtin_amdgcn_raw_buffer_load_b128(rd, voff2, so2 + i * (DM * 2), 0); SB(); }
  }
  const int kvoff = lane * 16;
  const int chunk = (b * HQ + h) * 2 + s;
  const int kvbase = chunk * (512 * 1024) + w * 4096;
  const int r0 = (xg * 32 + j) * 4;
#define KVOFF(t) (kvbase + ((((t) + r0) & 15) << 15))
  v4u a[16];
#pragma unroll
  for (int g = 0; g < 4; ++g) { const int o = KVOFF(g);
#pragma unroll
    for (int i = 0; i < 4; ++i) { a[g * 4 + i] = __builtin_amdgcn_raw_buffer_load_b128(rk, kvoff, o + i * 1024, AUX_NT); SB(); } }

  {
    float qa[8];
#pragma unroll
    for (int k = 0; k < 8; ++k) qa[k] = 0.f;
#pragma unroll
    for (int i = 0; i < 16; ++i) {
      const unsigned xd = i < 8 ? xq0[i >> 1] : xq1[(i - 8) >> 1];
      const unsigned xl = (i & 1) ? (xd >> 16) : (xd & 0xffffu), xh = (i & 1) ? (xd & 0xffff0000u) : (xd << 16);
#pragma unroll
      for (int k = 0; k < 4; ++k) { qa[2 * k] = dot2(wt[i][k], xl, qa[2 * k]); qa[2 * k + 1] = dot2(wt[i][k], xh, qa[2 * k + 1]); }
    }
#pragma unroll
    for (int k = 0; k < 8; ++k) qa[k] = xrow_sum(qa[k]);
    if (r == 0) {
      *(f32x4*)&lds[L_QP + w * 128 + c * 8] = f32x4{qa[0], qa[1], qa[2], qa[3]};
      *(f32x4*)&lds[L_QP + w * 128 + c * 8 + 4] = f32x4{qa[4], qa[5], qa[6], qa[7]};
    }
  }
  {
    float ga[8], ua[8];
#pragma unroll
    for (int k = 0; k < 8; ++k) { ga[k] = 0.f; ua[k] = 0.f; }
#pragma unroll
    for (int i = 0; i < 4; ++i) {
      const unsigned xd = xm[i >> 1];
      const unsigned xl = (i & 1) ? (xd >> 16) : (xd & 0xffffu), xh = (i & 1) ? (xd & 0xffff0000u) : (xd << 16);
#pragma unroll
      for (int k = 0; k < 4; ++k) {
        ga[2 * k] = dot2(mg[i][k], xl, ga[2 * k]); ga[2 * k + 1] = dot2(mg[i][k], xh, ga[2 * k + 1]);
        ua[2 * k] = dot2(mu[i][k], xl, ua[2 * k]); ua[2 * k + 1] = dot2(mu[i][k], xh, ua[2 * k + 1]);
      }
    }
#pragma unroll
    for (int k = 0; k < 8; ++k) {
      float g = ga[k]; g += dpp<ROR4>(g); g += dpp<XOR8>(g); ga[k] = xrow_sum(g);
      float u = ua[k]; u += dpp<ROR4>(u); u += dpp<XOR8>(u); ua[k] = xrow_sum(u);
    }
    if (lane < 4) {
      *(f32x4*)&lds[L_GU + w * 64 + lane * 8] = f32x4{ga[0], ga[1], ga[2], ga[3]};
      *(f32x4*)&lds[L_GU + w * 64 + lane * 8 + 4] = f32x4{ga[4], ga[5], ga[6], ga[7]};
      *(f32x4*)&lds[L_GU + w * 64 + 32 + lane * 8] = f32x4{ua[0], ua[1], ua[2], ua[3]};
      *(f32x4*)&lds[L_GU + w * 64 + 32 + lane * 8 + 4] = f32x4{ua[4], ua[5], ua[6], ua[7]};
    }
  }
  SB();
  {
    const int so = (h * DH + 16 * w) * (DM * 2);
#pragma unroll
    for (int i = 0; i < 16; ++i) { wt[i] = __builtin_amdgcn_raw_buffer_load_b128(ro, kvoff, so + i * (DM * 2), 0); SB(); }
  }
  __syncthreads();
  unsigned qpk[4];
  {
    f32x4 q0 = {0.f, 0.f, 0.f, 0.f}, q1 = q0;
#pragma unroll
    for (int ww = 0; ww < NW; ++ww) { q0 += *(const f32x4*)&lds[L_QP + ww * 128 + c * 8]; q1 += *(const f32x4*)&lds[L_QP + ww * 128 + c * 8 + 4]; }
    qpk[0] = pk_bf16(q0[0], q0[1]); qpk[1] = pk_bf16(q0[2], q0[3]); qpk[2] = pk_bf16(q1[0], q1[1]); qpk[3] = pk_bf16(q1[2], q1[3]);
    asm volatile("" : "+v"(qpk[0]), "+v"(qpk[1]), "+v"(qpk[2]), "+v"(qpk[3]));
    SB();
  }
  if (has_mlp) {
    f32x4 g4 = {0.f, 0.f, 0.f, 0.f}, u4 = g4;
    const int rr = lane >> 3;
#pragma unroll
    for (int ww = 0; ww < NW; ++ww) { g4 += *(const f32x4*)&lds[L_GU + ww * 64 + rr * 4]; u4 += *(const f32x4*)&lds[L_GU + ww * 64 + 32 + rr * 4]; }
    float ya[8];
#pragma unroll
    for (int k = 0; k < 8; ++k) ya[k] = 0.f;
#pragma unroll
    for (int i = 0; i < 4; ++i) {
      const float hv = rbf(rbf(sigmoidf_(rbf(g4[i]))) * rbf(u4[i]));
#pragma unroll
      for (int k = 0; k < 4; ++k) { ya[2 * k] = fmaf(hv, bflo(md[i][k]), ya[2 * k]); ya[2 * k + 1] = fmaf(hv, bfhi(md[i][k]), ya[2 * k + 1]); }
    }
#pragma unroll
    for (int k = 0; k < 8; ++k) { float y = ya[k]; y += dpp<XOR8>(y); ya[k] = xrow_sum(y); }
    if (lane < 8) {
      float* dst = ws + WS_YM + ((size_t)slice * NB + b) * DM + 64 * w + lane * 8;
      *(f32x4*)dst = f32x4{ya[0], ya[1], ya[2], ya[3]};
      *(f32x4*)(dst + 4) = f32x4{ya[4], ya[5], ya[6], ya[7]};
    }
  }
  SB();
  float* prow = &lds[L_P + (w * 4 + r) * 64];
  float mx = -INFINITY;
#pragma unroll 1
  for (int T = 0; T < 4; ++T) {
    const bool last = T == 3;
    const __amdgpu_buffer_rsrc_t rn = last ? rv : rk;
    const int tn = last ? 0 : 4 * T + 4;
    float xs[16];
#pragma unroll
    for (int g = 0; g < 4; ++g) { const int o = KVOFF(tn + g);
#pragma unroll
      for (int i = 0; i < 4; ++i) {
        v4u& kk = a[g * 4 + i];
        float d = dot2(kk[0], qpk[0], 0.f); d = dot2(kk[1], qpk[1], d); d = dot2(kk[2], qpk[2], d); d = dot2(kk[3], qpk[3], d);
        asm volatile("" : "+v"(d));
        xs[g * 4 + i] = d;
        SB();
        kk = __builtin_amdgcn_raw_buffer_load_b128(rn, kvoff, o + i * 1024, AUX_NT);
        SB();
      } }
#define TR_STEP(HALF, CTRL, BIT) _Pragma("unroll") for (int hh = 0; hh < (HALF); ++hh) { \
      const float lo_ = xs[hh], hi_ = xs[hh + (HALF)]; const float t1_ = lo_ + dpp<CTRL>(lo_), t2_ = hi_ + dpp<CTRL>(hi_); \
      xs[hh] = (c & (BIT)) ? t2_ : t1_; }
    TR_STEP(8, XOR8, 8) TR_STEP(4, XOR7, 4) TR_STEP(2, XOR2, 2) TR_STEP(1, XOR1, 1)
#undef TR_STEP
    const float sc = rbf(rbf(xs[0]) * rbf(SCALE)) * LOG2E;
    prow[16 * T + c] = sc;
    mx = fmaxf(mx, sc);
  }
  mx = xrow_max(row_max(mx));
  {
    float ls = 0.f;
#pragma unroll
    for (int T = 0; T < 4; ++T) { const float p = __builtin_amdgcn_exp2f(prow[16 * T + c] - mx); prow[16 * T + c] = p; ls += p; }
    ls = xrow_sum(row_sum(ls));
    if (lane == 0) { lds[L_ML + 2 * w] = mx; lds[L_ML + 2 * w + 1] = ls; }
  }
  asm volatile("s_waitcnt lgkmcnt(0)" ::: "memory");
  float oa[8];
#pragma unroll
  for (int k = 0; k < 8; ++k) oa[k] = 0.f;
#pragma unroll 1
  for (int T = 0; T < 4; ++T) {
    const int tn = 4 * T + 4;
#pragma unroll
    for (int g = 0; g < 4; ++g) {
      const int o = T == 3 ? 0x40000000 : KVOFF(tn + g);
      const f32x4 p4 = *(const f32x4*)&prow[16 * T + 4 * g];
#pragma unroll
      for (int i = 0; i < 4; ++i) {
        v4u& vv = a[g * 4 + i];
#pragma unroll
        for (int k = 0; k < 4; ++k) { oa[2 * k] = fmaf(p4[i], bflo(vv[k]), oa[2 * k]); oa[2 * k + 1] = fmaf(p4[i], bfhi(vv[k]), oa[2 * k + 1]); }
        asm volatile("" : "+v"(oa[0]), "+v"(oa[1]), "+v"(oa[2]), "+v"(oa[3]), "+v"(oa[4]), "+v"(oa[5]), "+v"(oa[6]), "+v"(oa[7]));
        SB();
        vv = __builtin_amdgcn_raw_buffer_load_b128(rv, kvoff, o + i * 1024, AUX_NT);
        SB();
      } }
  }
#pragma unroll
  for (int k = 0; k < 8; ++k) oa[k] = xrow_sum(oa[k]);
  if (r == 0) {
    *(f32x4*)&lds[L_OP + w * 128 + c * 8] = f32x4{oa[0], oa[1], oa[2], oa[3]};
    *(f32x4*)&lds[L_OP + w * 128 + c * 8 + 4] = f32x4{oa[4], oa[5], oa[6], oa[7]};
  }
  __syncthreads();
  float M = -INFINITY;
#pragma unroll
  for (int ww = 0; ww < NW; ++ww) M = fmaxf(M, lds[L_ML + 2 * ww]);
  float L = 0.f, od = 0.f;
#pragma unroll
  for (int ww = 0; ww < NW; ++ww) {
    const float f = __builtin_amdgcn_exp2f(lds[L_ML + 2 * ww] - M);
    L = fmaf(f, lds[L_ML + 2 * ww + 1], L);
    od = fmaf(f, lds[L_OP + ww * 128 + 16 * w + c], od);
  }
  float ya[8];
#pragma unroll
  for (int k = 0; k < 8; ++k) ya[k] = 0.f;
#pragma unroll
  for (int i = 0; i < 16; ++i) {
    const float ov = __builtin_bit_cast(float, __builtin_amdgcn_readlane(__builtin_bit_cast(int, od), i));
#pragma unroll
    for (int k = 0; k < 4; ++k) { ya[2 * k] = fmaf(ov, bflo(wt[i][k]), ya[2 * k]); ya[2 * k + 1] = fmaf(ov, bfhi(wt[i][k]), ya[2 * k + 1]); }
  }
  *(f32x4*)&lds[L_YP + w * 512 + lane * 8] = f32x4{ya[0], ya[1], ya[2], ya[3]};
  *(f32x4*)&lds[L_YP + w * 512 + lane * 8 + 4] = f32x4{ya[4], ya[5], ya[6], ya[7]};
  __syncthreads();
  float y = 0.f;
#pragma unroll
  for (int ww = 0; ww < NW; ++ww) y += lds[L_YP + ww * 512 + tid];
  ws[WS_Y + (size_t)chunk * DM + tid] = y;
  if (tid == 0) { ws[WS_ML + 2 * chunk] = M; ws[WS_ML + 2 * chunk + 1] = L; }
}

__global__ void __launch_bounds__(256)
k_comb(const unsigned short* __restrict__ x, const unsigned short* __restrict__ gw, const float* __restrict__ ws, unsigned short* __restrict__ out) {
  __shared__ __attribute__((aligned(16))) float red[2 * 4 * 64 + 4];
  const int tid = threadIdx.x, lane = tid & 63, w = tid >> 6, q = lane & 15, gp = tid >> 4;
  const int cb = blockIdx.x, b = blockIdx.y;
  const float* yb = ws + WS_Y + ((size_t)(b * HQ + 2 * gp) * 2) * DM + cb * 64 + q * 4;
  f32x4 y[4];
#pragma unroll
  for (int i = 0; i < 4; ++i) y[i] = *(const f32x4*)(yb + i * DM);
  const f32x4 ml0 = *(const f32x4*)(ws + WS_ML + (size_t)(b * HQ + 2 * gp) * 4), ml1 = *(const f32x4*)(ws + WS_ML + (size_t)(b * HQ + 2 * gp + 1) * 4);
  f32x4 ym[3];
#pragma unroll
  for (int i = 0; i < 3; ++i) { const int sl = gp + 16 * i; ym[i] = sl < NSL ? *(const f32x4*)(ws + WS_YM + ((size_t)sl * NB + b) * DM + cb * 64 + q * 4) : f32x4{0.f, 0.f, 0.f, 0.f}; }
  const unsigned xv = *(const unsigned*)(x + b * DM + 2 * tid), gv = *(const unsigned*)(gw + 2 * tid);
  float gd = rbf(bflo(xv) * bflo(gv)) + rbf(bfhi(xv) * bfhi(gv));
  gd = xrow_sum(row_sum(gd));
  f32x4 acc, accm = ym[0] + ym[1] + ym[2];
  {
    const float M0 = fmaxf(ml0[0], ml0[2]), e0 = __builtin_amdgcn_exp2f(ml0[0] - M0), e1 = __builtin_amdgcn_exp2f(ml0[2] - M0), i0 = 1.0f / (e0 * ml0[1] + e1 * ml0[3]);
    const float M1 = fmaxf(ml1[0], ml1[2]), e2 = __builtin_amdgcn_exp2f(ml1[0] - M1), e3 = __builtin_amdgcn_exp2f(ml1[2] - M1), i1 = 1.0f / (e2 * ml1[1] + e3 * ml1[3]);
    acc = y[0] * (e0 * i0) + y[1] * (e1 * i0) + y[2] * (e2 * i1) + y[3] * (e3 * i1);
  }
#pragma unroll
  for (int k = 0; k < 4; ++k) { acc[k] = xrow_sum(acc[k]); accm[k] = xrow_sum(accm[k]); }
  if (lane < 16) { *(f32x4*)&red[w * 64 + q * 4] = acc; *(f32x4*)&red[256 + w * 64 + q * 4] = accm; }
  if (lane == 0) red[512 + w] = gd;
  __syncthreads();
  if (tid < 64) {
    const float a = red[tid] + red[64 + tid] + red[128 + tid] + red[192 + tid];
    const float m = red[256 + tid] + red[320 + tid] + red[384 + tid] + red[448 + tid];
    const float z = red[512] + red[513] + red[514] + red[515];
    const float g = rbf(sigmoidf_(rbf(z)));
    const float o = rbf(g * rbf(a)) + rbf(rbf(1.0f - g) * rbf(m));
    out[b * DM + cb * 64 + tid] = __builtin_bit_cast(unsigned short, (__bf16)o);
  }
}
}

extern "C" void kernel_launch(void* const* d_in, const int* in_sizes, int n_in, void* d_out, int out_size, void* d_ws, size_t ws_size,
                              hipStream_t stream) {
  typedef const unsigned short* P;
  hipLaunchKernelGGL(k_main, dim3(NWG), dim3(NTHR), 0, stream, (P)d_in[0], (P)d_in[1], (P)d_in[2], (P)d_in[3], (P)d_in[4], (P)d_in[6], (P)d_in[7],
                     (P)d_in[8], (float*)d_ws);
  hipLaunchKernelGGL(k_comb, dim3(8, 4), dim3(256), 0, stream, (P)d_in[0], (P)d_in[9], (const float*)d_ws, (unsigned short*)d_out);
}
```

```cpp
#include <hip/hip_runtime.h>
#include <hip/hip_bf16.h>
#include <cstdint>

#define MIMIC 1

namespace {
typedef unsigned v4u __attribute__((ext_vector_type(4)));
typedef unsigned v2u __attribute__((ext_vector_type(2)));
typedef float f32x4 __attribute__((ext_vector_type(4)));
typedef __bf16 bf16x2 __attribute__((ext_vector_type(2)));

constexpr int NB = 4, DM = 512, HQ = 32, SK = 4096, DH = 128, FF = 1376;
constexpr int NWG = 256, NTHR = 512, NW = 8;
constexpr int NSL = 43;
constexpr int RSW3 = 0x00020000;
constexpr int AUX_NT = 2;
constexpr float LOG2E = 1.4426950408889634f;
constexpr float SCALE = 0.08838834764831845f;

constexpr size_t WS_Y = 0;
constexpr size_t WS_ML = WS_Y + 256 * 512;
constexpr size_t WS_YM = WS_ML + 512;

__device__ __forceinline__ float dot2(unsigned a, unsigned b, float acc) {
  return __builtin_amdgcn_fdot2_f32_bf16(__builtin_bit_cast(bf16x2, a), __builtin_bit_cast(bf16x2, b), acc, false);
}
__device__ __forceinline__ float bflo(unsigned u) { return __builtin_bit_cast(float, u << 16); }
__device__ __forceinline__ float bfhi(unsigned u) { return __builtin_bit_cast(float, u & 0xffff0000u); }
__device__ __forceinline__ unsigned pk_bf16(float lo, float hi) {
  bf16x2 r; r.x = (__bf16)lo; r.y = (__bf16)hi; return __builtin_bit_cast(unsigned, r);
}
__device__ __forceinline__ float rbf(float x) { return (float)(__bf16)x; }
template <int CTRL> __device__ __forceinline__ float dpp(float x) {
  return __builtin_bit_cast(float, __builtin_amdgcn_mov_dpp(__builtin_bit_cast(int, x), CTRL, 0xf, 0xf, true));
}
constexpr int XOR1 = 0xB1, XOR2 = 0x4E, XOR7 = 0x141, XOR8 = 0x128, ROR4 = 0x124;
__device__ __forceinline__ float xrow_sum(float x) {
  auto s = __builtin_amdgcn_permlane16_swap(__float_as_uint(x), __float_as_uint(x), false, false);
  x = __uint_as_float(s[0]) + __uint_as_float(s[1]);
  auto t = __builtin_amdgcn_permlane32_swap(__float_as_uint(x), __float_as_uint(x), false, false);
  return __uint_as_float(t[0]) + __uint_as_float(t[1]);
}
__device__ __forceinline__ float xrow_max(float x) {
  auto s = __builtin_amdgcn_permlane16_swap(__float_as_uint(x), __float_as_uint(x), false, false);
  x = fmaxf(__uint_as_float(s[0]), __uint_as_float(s[1]));
  auto t = __builtin_amdgcn_permlane32_swap(__float_as_uint(x), __float_as_uint(x), false, false);
  return fmaxf(__uint_as_float(t[0]), __uint_as_float(t[1]));
}
__device__ __forceinline__ float row_sum(float x) { x += dpp<XOR1>(x); x += dpp<XOR2>(x); x += dpp<XOR7>(x); x += dpp<XOR8>(x); return x; }
__device__ __forceinline__ float row_max(float x) { x = fmaxf(x, dpp<XOR1>(x)); x = fmaxf(x, dpp<XOR2>(x)); x = fmaxf(x, dpp<XOR7>(x)); x = fmaxf(x, dpp<XOR8>(x)); return x; }
__device__ __forceinline__ float sigmoidf_(float x) { return 1.0f / (1.0f + __expf(-x)); }
#define SB() __builtin_amdgcn_sched_barrier(0)
#define STAMP(k) do { } while (0)
#define AUX_WQ 2
#define EARLYK_AT 99
#define K0 8
#define RG 3
#define SCHED 1
#define ZQ 0
#define ZO 0
#define ZM 0
#define ROTMODE 1
#define ROT 4
template <int N> struct IC { static constexpr int value = N; };
#define AUX_W 0

constexpr int L_QP = 0;
constexpr int L_GU = L_QP + 8 * 128;
constexpr int L_P = L_GU + 8 * 64;
constexpr int L_ML = L_P + 8 * 256;
constexpr int L_OP = L_ML + 16;
constexpr int L_YP = L_OP + 8 * 128;
constexpr int L_END = L_YP + 8 * 512;

__global__ void __launch_bounds__(NTHR, 2)
k_main(const unsigned short* __restrict__ x, const unsigned short* __restrict__ kc, const unsigned short* __restrict__ vc,
       const unsigned short* __restrict__ wq, const unsigned short* __restrict__ wo, const unsigned short* __restrict__ wg,
       const unsigned short* __restrict__ wu, const unsigned short* __restrict__ wd, float* __restrict__ ws) {
  __shared__ __attribute__((aligned(16))) float lds[L_END];
  const int tid = threadIdx.x, lane = tid & 63, w = __builtin_amdgcn_readfirstlane(tid >> 6), r = lane >> 4, c = lane & 15;
  const int bid = blockIdx.x, xg = bid & 7, j = bid >> 3;
  const int h = xg * 4 + (j >> 3), b = (j >> 1) & 3, s = j & 1;
  const int slice = xg + 8 * ((j >> 3) * 2 + s);
  const bool has_mlp = slice < NSL;

  const __amdgpu_buffer_rsrc_t rk = __builtin_amdgcn_make_buffer_rsrc((void*)kc, 0, NB * HQ * SK * DH * 2, RSW3);
  const __amdgpu_buffer_rsrc_t rv = __builtin_amdgcn_make_buffer_rsrc((void*)vc, 0, NB * HQ * SK * DH * 2, RSW3);
  const __amdgpu_buffer_rsrc_t rq = __builtin_amdgcn_make_buffer_rsrc((void*)wq, 0, ZQ ? 0 : DM * HQ * DH * 2, RSW3);
  const __amdgpu_buffer_rsrc_t ro = __builtin_amdgcn_make_buffer_rsrc((void*)wo, 0, ZO ? 0 : DM * HQ * DH * 2, RSW3);
  const __amdgpu_buffer_rsrc_t rx = __builtin_amdgcn_make_buffer_rsrc((void*)x, 0, NB * DM * 2, RSW3);
  const int mlp_bytes = (has_mlp && !ZM) ? DM * FF * 2 : 0;
  const __amdgpu_buffer_rsrc_t rg = __builtin_amdgcn_make_buffer_rsrc((void*)wg, 0, mlp_bytes, RSW3);
  const __amdgpu_buffer_rsrc_t ru = __builtin_amdgcn_make_buffer_rsrc((void*)wu, 0, mlp_bytes, RSW3);
  const __amdgpu_buffer_rsrc_t rd = __builtin_amdgcn_make_buffer_rsrc((void*)wd, 0, mlp_bytes, RSW3);

  STAMP(0);
  const int m8 = j & 7;
  unsigned xd[8];
#pragma unroll
  for (int k = 0; k < 8; ++k) { xd[k] = __builtin_amdgcn_raw_buffer_load_b32(rx, r * 4, (b * DM + 64 * ((k + m8) & 7) + 8 * w) * 2, 0); }
  const v2u xm = __builtin_amdgcn_raw_buffer_load_b64(rx, (lane >> 2) * 8, (b * DM + 64 * w) * 2, 0);
  SB();
  const int kvoff = lane * 16;
  const int chunk = (b * HQ + h) * 2 + s;
  const int kvbase = chunk * (512 * 1024) + w * 4096;
  const int r0 = ROTMODE == 1 ? chunk * 4 : ROTMODE == 2 ? chunk : ROTMODE == 3 ? 0 : ROTMODE == 4 ? chunk * 4 + (chunk >> 2) : ROTMODE == 5 ? chunk * 8 : ROTMODE == 6 ? chunk * 2 : (chunk >> 1) * 4;
#define KVOFF(t) (kvbase + ((((t) + r0) & 15) << 15))
  v4u wq_[16], wt[16];
  v4u a[4 * RG];
  v4u mg[4], mu[4], md[4];
  const int gvoff = (lane >> 2) * 4 * (FF * 2) + (lane & 3) * 16, gso = 64 * w * (FF * 2) + slice * 64;
  const int dvoff = (lane >> 3) * 4 * (DM * 2) + (lane & 7) * 16, dso = slice * 32 * (DM * 2) + w * 128;
  const int oso = (h * DH + 16 * w) * (DM * 2);
#define KLOAD(n) { a[n] = __builtin_amdgcn_raw_buffer_load_b128(rk, kvoff, KVOFF((n) >> 2) + ((n) & 3) * 1024, AUX_NT); SB(); }
#define LD_G(i) { mg[i] = __builtin_amdgcn_raw_buffer_load_b128(rg, gvoff, gso + (((i) + b) & 3) * (FF * 2), AUX_W); SB(); }
#define LD_U(i) { mu[i] = __builtin_amdgcn_raw_buffer_load_b128(ru, gvoff, gso + (((i) + b) & 3) * (FF * 2), AUX_W); SB(); }
#define LD_D(i) { md[i] = __builtin_amdgcn_raw_buffer_load_b128(rd, dvoff, dso + (((i) + b) & 3) * (DM * 2), AUX_W); SB(); }
#define LD_O(i) { wt[i] = __builtin_amdgcn_raw_buffer_load_b128(ro, kvoff, oso + (((i) + 2 * m8) & 15) * (DM * 2), AUX_W); SB(); }
  const int qvoff = r * 2 * (HQ * DH * 2) + c * 16, qso = 8 * w * (HQ * DH * 2) + h * DH * 2;
#define LD_Q(i) { wq_[i] = __builtin_amdgcn_raw_buffer_load_b128(rq, qvoff, qso + (64 * ((((i) >> 1) + m8) & 7) + ((i) & 1)) * (HQ * DH * 2), AUX_WQ); SB(); }
  LD_Q(0) LD_Q(1) LD_O(0) LD_O(1) LD_G(0) LD_U(0) LD_D(0)
#pragma unroll
  for (int i = 2; i < 16; ++i) LD_Q(i)
  __builtin_amdgcn_s_barrier();
  SB();
#define BURST2() { _Pragma("unroll") for (int i = 2; i < 16; ++i) LD_O(i) \
  LD_G(1) LD_G(2) LD_G(3) LD_U(1) LD_U(2) LD_U(3) LD_D(1) LD_D(2) LD_D(3) \
  __builtin_amdgcn_s_barrier(); SB(); }
  STAMP(1);
#pragma unroll
  for (int n = 0; n < K0; ++n) KLOAD(n)

  {
    float qa[8];
#pragma unroll
    for (int k = 0; k < 8; ++k) qa[k] = 0.f;
#pragma unroll
    for (int i = 0; i < 16; ++i) {
      const unsigned xx = xd[i >> 1];
      const unsigned xl = (i & 1) ? (xx >> 16) : (xx & 0xffffu), xh = (i & 1) ? (xx & 0xffff0000u) : (xx << 16);
#pragma unroll
      for (int k = 0; k < 4; ++k) { qa[2 * k] = dot2(wq_[i][k], xl, qa[2 * k]); qa[2 * k + 1] = dot2(wq_[i][k], xh, qa[2 * k + 1]); }
    }
#pragma unroll
    for (int k = 0; k < 8; ++k) qa[k] = xrow_sum(qa[k]);
    asm volatile("" : "+v"(qa[0]), "+v"(qa[1]), "+v"(qa[2]), "+v"(qa[3]), "+v"(qa[4]), "+v"(qa[5]), "+v"(qa[6]), "+v"(qa[7]));
    if (r == 0) {
      *(f32x4*)&lds[L_QP + w * 128 + c * 8] = f32x4{qa[0], qa[1], qa[2], qa[3]};
      *(f32x4*)&lds[L_QP + w * 128 + c * 8 + 4] = f32x4{qa[4], qa[5], qa[6], qa[7]};
    }
  }
  SB();
  SB();
#pragma unroll
  for (int n = K0; n < 4 * RG; ++n) KLOAD(n)
  __syncthreads();
  STAMP(2);
  unsigned qpk[4];
  {
    f32x4 q0 = {0.f, 0.f, 0.f, 0.f}, q1 = q0;
#pragma unroll
    for (int ww = 0; ww < NW; ++ww) { q0 += *(const f32x4*)&lds[L_QP + ww * 128 + c * 8]; q1 += *(const f32x4*)&lds[L_QP + ww * 128 + c * 8 + 4]; }
    qpk[0] = pk_bf16(q0[0], q0[1]); qpk[1] = pk_bf16(q0[2], q0[3]); qpk[2] = pk_bf16(q1[0], q1[1]); qpk[3] = pk_bf16(q1[2], q1[3]);
    asm volatile("" : "+v"(qpk[0]), "+v"(qpk[1]), "+v"(qpk[2]), "+v"(qpk[3]));
    SB();
  }
  SB();
  STAMP(3);
  float* prow = &lds[L_P + (w * 4 + r) * 64];
  float sc[4];
#define TR_STEP(HALF, CTRL, BIT) _Pragma("unroll") for (int hh = 0; hh < (HALF); ++hh) { \
      const float lo_ = xs[hh], hi_ = xs[hh + (HALF)]; const float t1_ = lo_ + dpp<CTRL>(lo_), t2_ = hi_ + dpp<CTRL>(hi_); \
      xs[hh] = (c & (BIT)) ? t2_ : t1_; }
#define SCORE(v) (rbf(rbf(v) * rbf(SCALE)) * LOG2E)
#define KGROUP(T, g) { constexpr int un = 4 * T + g + RG, sl = ((4 * T + g) % RG) * 4; const int o = KVOFF(un & 15); \
      _Pragma("unroll") for (int i = 0; i < 4; ++i) { \
        v4u& kk = a[sl + i]; \
        float d = dot2(kk[0], qpk[0], 0.f); d = dot2(kk[1], qpk[1], d); d = dot2(kk[2], qpk[2], d); d = dot2(kk[3], qpk[3], d); \
        asm volatile("" : "+v"(d)); \
        xs[g * 4 + i] = d; \
        SB(); \
        kk = __builtin_amdgcn_raw_buffer_load_b128(un < 16 ? rk : rv, kvoff, o + i * 1024, AUX_NT); \
        SB(); \
      } \
      }
#define KSTEP(T) { \
    float xs[16]; \
    KGROUP(T, 0) KGROUP(T, 1) KGROUP(T, 2) KGROUP(T, 3) \
    TR_STEP(8, XOR8, 8) TR_STEP(4, XOR7, 4) TR_STEP(2, XOR2, 2) TR_STEP(1, XOR1, 1) \
    sc[T] = SCORE(xs[0]); \
    asm volatile("" : "+v"(sc[T])); \
    SB(); }
  KSTEP(0)
  STAMP(4);
  KSTEP(1)
  KSTEP(2)
  KSTEP(3)
  {
    const float mx = xrow_max(row_max(fmaxf(fmaxf(sc[0], sc[1]), fmaxf(sc[2], sc[3]))));
    float ls = 0.f;
#pragma unroll
    for (int T = 0; T < 4; ++T) { const float p = __builtin_amdgcn_exp2f(sc[T] - mx); prow[16 * T + c] = p; ls += p; }
    ls = xrow_sum(row_sum(ls));
    if (lane == 0) { lds[L_ML + 2 * w] = mx; lds[L_ML + 2 * w + 1] = ls; }
  }
  __syncthreads();
  float ym[8];
  BURST2()
  SB();
  STAMP(5);
  float oa[8];
#pragma unroll
  for (int k = 0; k < 8; ++k) oa[k] = 0.f;
#define VGROUP(T, g) { \
      constexpr int un = 4 * T + g + RG, sl = ((16 + 4 * T + g) % RG) * 4; const int o = KVOFF(un & 15); \
      const f32x4 p4 = *(const f32x4*)&prow[16 * T + 4 * g]; \
      _Pragma("unroll") for (int i = 0; i < 4; ++i) { \
        v4u& vv = a[sl + i]; \
        _Pragma("unroll") for (int k = 0; k < 4; ++k) { oa[2 * k] = fmaf(p4[i], bflo(vv[k]), oa[2 * k]); oa[2 * k + 1] = fmaf(p4[i], bfhi(vv[k]), oa[2 * k + 1]); } \
        asm volatile("" : "+v"(oa[0]), "+v"(oa[1]), "+v"(oa[2]), "+v"(oa[3]), "+v"(oa[4]), "+v"(oa[5]), "+v"(oa[6]), "+v"(oa[7])); \
        SB(); \
        if (un < 16) vv = __builtin_amdgcn_raw_buffer_load_b128(rv, kvoff, o + i * 1024, AUX_NT); \
        SB(); \
      } \
      }
#define VSTEP(T) VGROUP(T, 0) VGROUP(T, 1) VGROUP(T, 2) VGROUP(T, 3)
  VSTEP(0)
  {
    float ga[8], ua[8];
#pragma unroll
    for (int k = 0; k < 8; ++k) { ga[k] = 0.f; ua[k] = 0.f; }
#pragma unroll
    for (int i = 0; i < 4; ++i) {
      const int ri = (i + b) & 3;
      const unsigned xx = (ri & 2) ? xm[1] : xm[0];
      const unsigned xl = (ri & 1) ? (xx >> 16) : (xx & 0xffffu), xh = xl << 16;
#pragma unroll
      for (int k = 0; k < 4; ++k) {
        ga[2 * k] = dot2(mg[i][k], xl, ga[2 * k]); ga[2 * k + 1] = dot2(mg[i][k], xh, ga[2 * k + 1]);
        ua[2 * k] = dot2(mu[i][k], xl, ua[2 * k]); ua[2 * k + 1] = dot2(mu[i][k], xh, ua[2 * k + 1]);
      }
    }
#pragma unroll
    for (int k = 0; k < 8; ++k) {
      float g = ga[k]; g += dpp<ROR4>(g); g += dpp<XOR8>(g); ga[k] = xrow_sum(g);
      float u = ua[k]; u += dpp<ROR4>(u); u += dpp<XOR8>(u); ua[k] = xrow_sum(u);
    }
    asm volatile("" : "+v"(ga[0]), "+v"(ga[1]), "+v"(ga[2]), "+v"(ga[3]), "+v"(ga[4]), "+v"(ga[5]), "+v"(ga[6]), "+v"(ga[7]));
    asm volatile("" : "+v"(ua[0]), "+v"(ua[1]), "+v"(ua[2]), "+v"(ua[3]), "+v"(ua[4]), "+v"(ua[5]), "+v"(ua[6]), "+v"(ua[7]));
    if (lane < 4) {
      *(f32x4*)&lds[L_GU + w * 64 + lane * 8] = f32x4{ga[0], ga[1], ga[2], ga[3]};
      *(f32x4*)&lds[L_GU + w * 64 + lane * 8 + 4] = f32x4{ga[4], ga[5], ga[6], ga[7]};
      *(f32x4*)&lds[L_GU + w * 64 + 32 + lane * 8] = f32x4{ua[0], ua[1], ua[2], ua[3]};
      *(f32x4*)&lds[L_GU + w * 64 + 32 + lane * 8 + 4] = f32x4{ua[4], ua[5], ua[6], ua[7]};
    }
  }
  SB();
  VSTEP(1)
  __syncthreads();
  {
    f32x4 g4 = {0.f, 0.f, 0.f, 0.f}, u4 = g4;
    const int rr = lane >> 3;
#pragma unroll
    for (int ww = 0; ww < NW; ++ww) { g4 += *(const f32x4*)&lds[L_GU + ww * 64 + rr * 4]; u4 += *(const f32x4*)&lds[L_GU + ww * 64 + 32 + rr * 4]; if (ww & 1) { asm volatile("" : "+v"(g4), "+v"(u4)); SB(); } }
#pragma unroll
    for (int k = 0; k < 8; ++k) ym[k] = 0.f;
#pragma unroll
    for (int i = 0; i < 4; ++i) {
      const int ri = (i + b) & 3;
      const float gi = ri == 0 ? g4[0] : ri == 1 ? g4[1] : ri == 2 ? g4[2] : g4[3], ui = ri == 0 ? u4[0] : ri == 1 ? u4[1] : ri == 2 ? u4[2] : u4[3];
      const float hv = rbf(rbf(sigmoidf_(rbf(gi))) * rbf(ui));
#pragma unroll
      for (int k = 0; k < 4; ++k) { ym[2 * k] = fmaf(hv, bflo(md[i][k]), ym[2 * k]); ym[2 * k + 1] = fmaf(hv, bfhi(md[i][k]), ym[2 * k + 1]); }
    }
#pragma unroll
    for (int k = 0; k < 8; ++k) { float y = ym[k]; y += dpp<XOR8>(y); ym[k] = xrow_sum(y); }
    asm volatile("" : "+v"(ym[0]), "+v"(ym[1]), "+v"(ym[2]), "+v"(ym[3]), "+v"(ym[4]), "+v"(ym[5]), "+v"(ym[6]), "+v"(ym[7]));
  }
  SB();
  VSTEP(2) VSTEP(3)
  if (has_mlp && lane < 8) {
    float* dst = ws + WS_YM + ((size_t)slice * NB + b) * DM + 64 * w + lane * 8;
    *(f32x4*)dst = f32x4{ym[0], ym[1], ym[2], ym[3]};
    *(f32x4*)(dst + 4) = f32x4{ym[4], ym[5], ym[6], ym[7]};
  }
#pragma unroll
  for (int k = 0; k < 8; ++k) oa[k] = xrow_sum(oa[k]);
  STAMP(6);
  if (r == 0) {
    *(f32x4*)&lds[L_OP + w * 128 + c * 8] = f32x4{oa[0], oa[1], oa[2], oa[3]};
    *(f32x4*)&lds[L_OP + w * 128 + c * 8 + 4] = f32x4{oa[4], oa[5], oa[6], oa[7]};
  }
  __syncthreads();
  float M = -INFINITY;
#pragma unroll
  for (int ww = 0; ww < NW; ++ww) M = fmaxf(M, lds[L_ML + 2 * ww]);
  float L = 0.f, od = 0.f;
#pragma unroll
  for (int ww = 0; ww < NW; ++ww) {
    const float f = __builtin_amdgcn_exp2f(lds[L_ML + 2 * ww] - M);
    L = fmaf(f, lds[L_ML + 2 * ww + 1], L);
    od = fmaf(f, lds[L_OP + ww * 128 + 16 * w + c], od);
  }
  float ya[8];
#pragma unroll
  for (int k = 0; k < 8; ++k) ya[k] = 0.f;
#pragma unroll
  for (int i = 0; i < 16; ++i) {
    const float ov = __builtin_bit_cast(float, __builtin_amdgcn_readlane(__builtin_bit_cast(int, od), (i + 2 * m8) & 15));
#pragma unroll
    for (int k = 0; k < 4; ++k) { ya[2 * k] = fmaf(ov, bflo(wt[i][k]), ya[2 * k]); ya[2 * k + 1] = fmaf(ov, bfhi(wt[i][k]), ya[2 * k + 1]); }
  }
  *(f32x4*)&lds[L_YP + w * 512 + lane * 8] = f32x4{ya[0], ya[1], ya[2], ya[3]};
  *(f32x4*)&lds[L_YP + w * 512 + lane * 8 + 4] = f32x4{ya[4], ya[5], ya[6], ya[7]};
  __syncthreads();
  float y = 0.f;
#pragma unroll
  for (int ww = 0; ww < NW; ++ww) y += lds[L_YP + ww * 512 + tid];
  ws[WS_Y + (size_t)chunk * DM + tid] = y;
  if (tid == 0) { ws[WS_ML + 2 * chunk] = M; ws[WS_ML + 2 * chunk + 1] = L; }
}

__global__ void __launch_bounds__(256)
k_comb(const unsigned short* __restrict__ x, const unsigned short* __restrict__ gw, const float* __restrict__ ws, unsigned short* __restrict__ out) {
  __shared__ __attribute__((aligned(16))) float red[2 * 4 * 64 + 4];
  const int tid = threadIdx.x, lane = tid & 63, w = tid >> 6, q = lane & 15, gp = tid >> 4;
  const int cb = blockIdx.x, b = blockIdx.y;
  const float* yb = ws + WS_Y + ((size_t)(b * HQ + 2 * gp) * 2) * DM + cb * 64 + q * 4;
  f32x4 y[4];
#pragma unroll
  for (int i = 0; i < 4; ++i) y[i] = *(const f32x4*)(yb + i * DM);
  const f32x4 ml0 = *(const f32x4*)(ws + WS_ML + (size_t)(b * HQ + 2 * gp) * 4), ml1 = *(const f32x4*)(ws + WS_ML + (size_t)(b * HQ + 2 * gp + 1) * 4);
  f32x4 ym[3];
#pragma unroll
  for (int i = 0; i < 3; ++i) { const int sl = gp + 16 * i; ym[i] = sl < NSL ? *(const f32x4*)(ws + WS_YM + ((size_t)sl * NB + b) * DM + cb * 64 + q * 4) : f32x4{0.f, 0.f, 0.f, 0.f}; }
  const unsigned xv = *(const unsigned*)(x + b * DM + 2 * tid), gv = *(const unsigned*)(gw + 2 * tid);
  float gd = rbf(bflo(xv) * bflo(gv)) + rbf(bfhi(xv) * bfhi(gv));
  gd = xrow_sum(row_sum(gd));
  f32x4 acc, accm = ym[0] + ym[1] + ym[2];
  {
    const float M0 = fmaxf(ml0[0], ml0[2]), e0 = __builtin_amdgcn_exp2f(ml0[0] - M0), e1 = __builtin_amdgcn_exp2f(ml0[2] - M0), i0 = 1.0f / (e0 * ml0[1] + e1 * ml0[3]);
    const float M1 = fmaxf(ml1[0], ml1[2]), e2 = __builtin_amdgcn_exp2f(ml1[0] - M1), e3 = __builtin_amdgcn_exp2f(ml1[2] - M1), i1 = 1.0f / (e2 * ml1[1] + e3 * ml1[3]);
    acc = y[0] * (e0 * i0) + y[1] * (e1 * i0) + y[2] * (e2 * i1) + y[3] * (e3 * i1);
  }
#pragma unroll
  for (int k = 0; k < 4; ++k) { acc[k] = xrow_sum(acc[k]); accm[k] = xrow_sum(accm[k]); }
  if (lane < 16) { *(f32x4*)&red[w * 64 + q * 4] = acc; *(f32x4*)&red[256 + w * 64 + q * 4] = accm; }
  if (lane == 0) red[512 + w] = gd;
  __syncthreads();
  if (tid < 64) {
    const float a = red[tid] + red[64 + tid] + red[128 + tid] + red[192 + tid];
    const float m = red[256 + tid] + red[320 + tid] + red[384 + tid] + red[448 + tid];
    const float z = red[512] + red[513] + red[514] + red[515];
    const float g = rbf(sigmoidf_(rbf(z)));
    const float o = rbf(g * rbf(a)) + rbf(rbf(1.0f - g) * rbf(m));
    out[b * DM + cb * 64 + tid] = __builtin_bit_cast(unsigned short, (__bf16)o);
  }
}
}

extern "C" void kernel_launch(void* const* d_in, const int* in_sizes, int n_in, void* d_out, int out_size, void* d_ws, size_t ws_size,
                              hipStream_t stream) {
  typedef const unsigned short* P;
  hipLaunchKernelGGL(k_main, dim3(NWG), dim3(NTHR), 0, stream, (P)d_in[0], (P)d_in[1], (P)d_in[2], (P)d_in[3], (P)d_in[4], (P)d_in[6], (P)d_in[7],
                     (P)d_in[8], (float*)d_ws);
  hipLaunchKernelGGL(k_comb, dim3(8, 4), dim3(256), 0, stream, (P)d_in[0], (P)d_in[9], (const float*)d_ws, (unsigned short*)d_out);
}
```

```cpp
#include <hip/hip_runtime.h>
#include <hip/hip_bf16.h>
#include <cstdint>

namespace {
typedef unsigned v4u __attribute__((ext_vector_type(4)));
typedef unsigned v2u __attribute__((ext_vector_type(2)));
typedef float f32x4 __attribute__((ext_vector_type(4)));
typedef __bf16 bf16x2 __attribute__((ext_vector_type(2)));
#define GAS __attribute__((address_space(1)))
typedef GAS unsigned gu32;
typedef GAS unsigned long long gu64;
#define RLX_AGENT __ATOMIC_RELAXED, __HIP_MEMORY_SCOPE_AGENT

constexpr int NB = 4, DM = 512, HQ = 32, SK = 4096, DH = 128, FF = 1376;
constexpr int NWG = 256, NW = 8, NTHR = 64 * (NW + 1);
constexpr int NSL = 43;
constexpr int RSW3 = 0x00020000;
constexpr int AUX_NT = 2, AUX_SC1 = 16;
constexpr float LOG2E = 1.4426950408889634f;
constexpr float SCALE = 0.08838834764831845f;
constexpr unsigned SENT = 0xFFFFFFFFu;
constexpr unsigned SPIN_CAP = 1u << 18;

#define AUX_WQ 0
#define AUX_W 0
#define RG 4
#define AUX_K 2
#define AUX_V 0
#define K0 8

constexpr int WS_R = 0;
constexpr int WS_E = 4096;
constexpr int WS_CONF = 8192;
constexpr int WS_ML = 12288;
constexpr int WS_Y = 16384;
constexpr int WS_YM = WS_Y + 256 * 2048;
constexpr int WS_GU = WS_YM + NSL * 8192;
constexpr int WS_END = WS_GU + 22 * 4096;

__device__ __forceinline__ float dot2(unsigned a, unsigned b, float acc) {
  return __builtin_amdgcn_fdot2_f32_bf16(__builtin_bit_cast(bf16x2, a), __builtin_bit_cast(bf16x2, b), acc, false);
}
__device__ __forceinline__ float bflo(unsigned u) { return __builtin_bit_cast(float, u << 16); }
__device__ __forceinline__ float bfhi(unsigned u) { return __builtin_bit_cast(float, u & 0xffff0000u); }
__device__ __forceinline__ unsigned pk_bf16(float lo, float hi) {
  bf16x2 r; r.x = (__bf16)lo; r.y = (__bf16)hi; return __builtin_bit_cast(unsigned, r);
}
template <int CTRL> __device__ __forceinline__ float dpp(float x) {
  return __builtin_bit_cast(float, __builtin_amdgcn_mov_dpp(__builtin_bit_cast(int, x), CTRL, 0xf, 0xf, true));
}
constexpr int XOR1 = 0xB1, XOR2 = 0x4E, XOR7 = 0x141, XOR8 = 0x128, ROR4 = 0x124;
__device__ __forceinline__ float xrow_sum(float x) {
  auto s = __builtin_amdgcn_permlane16_swap(__float_as_uint(x), __float_as_uint(x), false, false);
  x = __uint_as_float(s[0]) + __uint_as_float(s[1]);
  auto t = __builtin_amdgcn_permlane32_swap(__float_as_uint(x), __float_as_uint(x), false, false);
  return __uint_as_float(t[0]) + __uint_as_float(t[1]);
}
__device__ __forceinline__ float xrow_max(float x) {
  auto s = __builtin_amdgcn_permlane16_swap(__float_as_uint(x), __float_as_uint(x), false, false);
  x = fmaxf(__uint_as_float(s[0]), __uint_as_float(s[1]));
  auto t = __builtin_amdgcn_permlane32_swap(__float_as_uint(x), __float_as_uint(x), false, false);
  return fmaxf(__uint_as_float(t[0]), __uint_as_float(t[1]));
}
__device__ __forceinline__ float row_sum(float x) { x += dpp<XOR1>(x); x += dpp<XOR2>(x); x += dpp<XOR7>(x); x += dpp<XOR8>(x); return x; }
__device__ __forceinline__ float row_max(float x) { x = fmaxf(x, dpp<XOR1>(x)); x = fmaxf(x, dpp<XOR2>(x)); x = fmaxf(x, dpp<XOR7>(x)); x = fmaxf(x, dpp<XOR8>(x)); return x; }
__device__ __forceinline__ float sigmoidf_(float x) { return 1.0f / (1.0f + __expf(-x)); }
__device__ __forceinline__ unsigned canon(float v) { const unsigned u = __float_as_uint(v); return u == SENT ? 0x7FC00000u : u; }
#define SB() __builtin_amdgcn_sched_barrier(0)
#define POLL_FENCE() asm volatile("" ::: "memory")
#define DRAIN() asm volatile("s_waitcnt vmcnt(0)" ::: "memory")
#define BAR() do { asm volatile("s_waitcnt lgkmcnt(0)" ::: "memory"); __builtin_amdgcn_s_barrier(); asm volatile("" ::: "memory"); } while (0)
#define KEEP8(a) asm volatile("" : "+v"(a[0]), "+v"(a[1]), "+v"(a[2]), "+v"(a[3]), "+v"(a[4]), "+v"(a[5]), "+v"(a[6]), "+v"(a[7]))
#define STAMP(k) do { } while (0)

constexpr int L_QP = 0;
constexpr int L_P = L_QP + 8 * 128;
constexpr int L_M = L_P + 8 * 256;
constexpr int L_L = L_M + 8;
constexpr int L_SC = L_L + 8;
constexpr int L_OP = L_SC + 8;
constexpr int L_YP = L_OP + 8 * 128;
constexpr int L_XT = L_YP + 8 * 512;
constexpr int L_END = L_XT + 272 * 4;
typedef __attribute__((address_space(3))) unsigned lu32;
__device__ __forceinline__ void lds_arrive(float* p) { asm volatile("s_waitcnt lgkmcnt(0)" ::: "memory"); __hip_atomic_fetch_add((lu32*)p, 1u, __ATOMIC_RELAXED, __HIP_MEMORY_SCOPE_WORKGROUP); }
__device__ __forceinline__ void lds_wait(float* p, unsigned n) {
#pragma unroll 1
  for (unsigned spin = 0; spin < (SPIN_CAP << 4); ++spin) {
    if (__builtin_amdgcn_readfirstlane(__hip_atomic_load((lu32*)p, __ATOMIC_RELAXED, __HIP_MEMORY_SCOPE_WORKGROUP)) >= n) break;
    __builtin_amdgcn_s_sleep(1);
  }
  asm volatile("" ::: "memory");
}

__global__ void __launch_bounds__(NTHR, 3)
k_main(const unsigned short* __restrict__ x, const unsigned short* __restrict__ kc, const unsigned short* __restrict__ vc,
       const unsigned short* __restrict__ wq, const unsigned short* __restrict__ wo, const unsigned short* __restrict__ wg,
       const unsigned short* __restrict__ wu, const unsigned short* __restrict__ wd, const unsigned short* __restrict__ gw,
       unsigned short* __restrict__ out, unsigned char* __restrict__ ws) {
  __shared__ __attribute__((aligned(16))) float lds[L_END];
  const int tid = threadIdx.x, lane = tid & 63, w = __builtin_amdgcn_readfirstlane(tid >> 6), r = lane >> 4, c = lane & 15;
  const int bid = blockIdx.x, xg = bid & 7, j = bid >> 3;
  const int h = xg * 4 + (j >> 3), b = (j >> 1) & 3, s = j & 1, m8 = j & 7;
  const int chunk = (b * HQ + h) * 2 + s;
  const __amdgpu_buffer_rsrc_t rx = __builtin_amdgcn_make_buffer_rsrc((void*)x, 0, NB * DM * 2, RSW3);
  const __amdgpu_buffer_rsrc_t rw = __builtin_amdgcn_make_buffer_rsrc((void*)ws, 0, WS_END, RSW3);
  STAMP(0);

  if (w == NW) {
    gu32* R = (gu32*)(ws + WS_R); gu32* E = (gu32*)(ws + WS_E); gu32* CONF = (gu32*)(ws + WS_CONF); gu64* ML = (gu64*)(ws + WS_ML);
    __builtin_amdgcn_s_setprio(3);
    if (lane < 3) ((unsigned*)lds)[L_SC + 1 + lane] = 0u;
    const int rho = (bid & 1) ? (bid < 8 ? 127 + (bid >> 1) : 999) : (bid >> 1) - 1;
    const bool gu = (unsigned)rho < 88u, dn = rho >= 88 && rho < 88 + NSL;
    const int sl = rho - 88;
    const unsigned r_old = __hip_atomic_load(R + chunk, RLX_AGENT), e_old = __hip_atomic_load(E + chunk, RLX_AGENT);
    v4u xv[4];
#pragma unroll
    for (int bb = 0; bb < 4; ++bb) xv[bb] = __builtin_amdgcn_raw_buffer_load_b128(rx, lane * 16, bb * DM * 2, 0);
    const v4u gv = *(const GAS v4u*)((const GAS unsigned char*)gw + lane * 16);
    const v4u sent4 = {SENT, SENT, SENT, SENT};
    __builtin_amdgcn_raw_buffer_store_b128(sent4, rw, lane * 16, WS_Y + chunk * 2048, AUX_SC1);
    __builtin_amdgcn_raw_buffer_store_b128(sent4, rw, lane * 16, WS_Y + chunk * 2048 + 1024, AUX_SC1);
    if (dn) {
#pragma unroll 1
      for (int i = 0; i < 8; ++i) __builtin_amdgcn_raw_buffer_store_b128(sent4, rw, lane * 16, WS_YM + sl * 8192 + i * 1024, AUX_SC1);
    }
    if (gu) __builtin_amdgcn_raw_buffer_store_b128(sent4, rw, lane * 16, WS_GU + rho * 1024, AUX_SC1);
    if (lane == 0) __hip_atomic_store(ML + chunk, ~0ull, RLX_AGENT);
    if (bid == 0 && lane == 0) __hip_atomic_store(CONF, 0u, RLX_AGENT);
    unsigned nonce = r_old + 1u; if (e_old - nonce < 2u) nonce += 2u;
    DRAIN();
    if (lane == 0) { if (bid != 0) __hip_atomic_store(R + chunk, nonce, RLX_AGENT); else __hip_atomic_fetch_add(CONF, 1u, RLX_AGENT); }
    float gate[4];
#pragma unroll
    for (int bb = 0; bb < 4; ++bb) {
      float gd = dot2(xv[bb][0], gv[0], 0.f); gd = dot2(xv[bb][1], gv[1], gd); gd = dot2(xv[bb][2], gv[2], gd); gd = dot2(xv[bb][3], gv[3], gd);
      gate[bb] = sigmoidf_(xrow_sum(row_sum(gd)));
    }
    const float gate_b = b == 0 ? gate[0] : b == 1 ? gate[1] : b == 2 ? gate[2] : gate[3];
#pragma unroll
    for (int k = 0; k < 4; ++k) *(v4u*)&lds[L_XT + (4 * lane + k) * 4] = v4u{xv[0][k], xv[1][k], xv[2][k], xv[3][k]};
#define ECHO() do { POLL_FENCE(); const v4u rq_ = __builtin_amdgcn_raw_buffer_load_b128(rw, lane * 16, WS_R, AUX_SC1); \
      __builtin_amdgcn_raw_buffer_store_b128(rq_, rw, lane * 16, WS_E, AUX_SC1); } while (0)
    if (bid == 0) ECHO();
    BAR();
    if (bid == 0) ECHO();
    BAR();
    const __amdgpu_buffer_rsrc_t rm = __builtin_amdgcn_make_buffer_rsrc((void*)(dn ? wd : (rho & 2) ? wu : wg), 0, (gu || dn) ? DM * FF * 2 : 0, RSW3);
    const int rowb = dn ? DM * 2 : FF * 2;
    const int mvo = dn ? lane * 16 : (lane >> 3) * 2 * (FF * 2) + (lane & 7) * 16;
    const int mso = dn ? sl * 32 * (DM * 2) : (rho & 1) * 256 * (FF * 2) + (rho >> 2) * 128, istep = dn ? 2 * (DM * 2) : 16 * (FF * 2);
    v4u m0[8], m1[8];
#pragma unroll
    for (int i = 0; i < 8; ++i) {
      m0[i] = __builtin_amdgcn_raw_buffer_load_b128(rm, mvo, mso + i * istep, 0); SB();
      m1[i] = __builtin_amdgcn_raw_buffer_load_b128(rm, mvo, mso + i * istep + rowb, 0); SB();
    }
    if (bid == 0) {
#pragma unroll 1
      for (unsigned spin = 0; spin < SPIN_CAP; ++spin) {
        POLL_FENCE();
        const unsigned cf = __hip_atomic_load(CONF, RLX_AGENT);
        asm volatile("s_waitcnt vmcnt(0)" : "+v"(const_cast<unsigned&>(cf)) :: "memory");
        v4u rq = __builtin_amdgcn_raw_buffer_load_b128(rw, lane * 16, WS_R, AUX_SC1);
        const bool all = __builtin_amdgcn_readfirstlane(cf) >= (unsigned)NWG;
        if (all) rq += 1u;
        __builtin_amdgcn_raw_buffer_store_b128(rq, rw, lane * 16, WS_E, AUX_SC1);
        if (all) break;
        __builtin_amdgcn_s_sleep(16);
      }
    } else {
#pragma unroll 1
      for (unsigned spin = 0; spin < SPIN_CAP; ++spin) {
        POLL_FENCE();
        if (__builtin_amdgcn_readfirstlane(__hip_atomic_load(E + chunk, RLX_AGENT)) == nonce) break;
        __builtin_amdgcn_s_sleep(32);
      }
      if (lane == 0) __hip_atomic_fetch_add(CONF, 1u, RLX_AGENT);
    }
    STAMP(4);
#define WAIT_GO() do { if (bid != 0) { _Pragma("unroll 1") for (unsigned spin = 0; spin < SPIN_CAP; ++spin) { POLL_FENCE(); \
        if (__builtin_amdgcn_readfirstlane(__hip_atomic_load(E + chunk, RLX_AGENT)) == nonce + 1u) break; \
        __builtin_amdgcn_s_sleep(32); } } } while (0)
    if (gu || dn) {
      if (dn) {
        lds_wait(&lds[L_SC + 3], 3);
        WAIT_GO();
        v2u pv[4];
        const int go = WS_GU + (sl >> 1) * 4096 + (sl & 1) * 128, gvo = r * 256 + c * 8;
#pragma unroll 1
        for (unsigned spin = 0; spin < SPIN_CAP; ++spin) {
          POLL_FENCE();
          bool ok = true;
#pragma unroll
          for (int k = 0; k < 4; ++k) { pv[k] = __builtin_amdgcn_raw_buffer_load_b64(rw, gvo, go + k * 1024, AUX_SC1); }
#pragma unroll
          for (int k = 0; k < 4; ++k) ok = ok && pv[k][0] != SENT && pv[k][1] != SENT;
          if (__all(ok)) break;
          __builtin_amdgcn_s_sleep(32);
        }
        const float g0 = __uint_as_float(pv[0][0]) + __uint_as_float(pv[1][0]), g1 = __uint_as_float(pv[0][1]) + __uint_as_float(pv[1][1]);
        const float u0 = __uint_as_float(pv[2][0]) + __uint_as_float(pv[3][0]), u1 = __uint_as_float(pv[2][1]) + __uint_as_float(pv[3][1]);
        ((unsigned*)lds)[L_XT + (256 + c) * 4 + r] = pk_bf16(sigmoidf_(g0) * u0, sigmoidf_(g1) * u1);
        asm volatile("s_waitcnt lgkmcnt(0)" ::: "memory");
      }
      const float* xt = &lds[L_XT + (dn ? 256 * 4 : ((rho & 1) * 128 + (lane >> 3)) * 4)];
      const int xstep = dn ? 4 : 32;
      float acc[4][8];
#pragma unroll
      for (int bb = 0; bb < 4; ++bb)
#pragma unroll
        for (int k = 0; k < 8; ++k) acc[bb][k] = 0.f;
#pragma unroll 1
      for (int I0 = 0; I0 < 16; I0 += 8) {
#pragma unroll
        for (int i = 0; i < 8; ++i) {
          const v4u xp = *(const v4u*)(xt + (I0 + i) * xstep);
#pragma unroll
          for (int k = 0; k < 4; ++k) {
            const unsigned lo = __builtin_amdgcn_perm(m1[i][k], m0[i][k], 0x05040100u), hi = __builtin_amdgcn_perm(m1[i][k], m0[i][k], 0x07060302u);
#pragma unroll
            for (int bb = 0; bb < 4; ++bb) { acc[bb][2 * k] = dot2(lo, xp[bb], acc[bb][2 * k]); acc[bb][2 * k + 1] = dot2(hi, xp[bb], acc[bb][2 * k + 1]); }
          }
          KEEP8(acc[0]); KEEP8(acc[1]); KEEP8(acc[2]); KEEP8(acc[3]);
          SB();
          const int o = I0 == 0 ? mso + (8 + i) * istep : 0x40000000;
          m0[i] = __builtin_amdgcn_raw_buffer_load_b128(rm, mvo, o, 0); SB();
          m1[i] = __builtin_amdgcn_raw_buffer_load_b128(rm, mvo, o + rowb, 0); SB();
        }
      }
      if (!dn) {
#pragma unroll
        for (int bb = 0; bb < 4; ++bb) {
          *(f32x4*)&lds[L_YP + lane * 32 + bb * 8] = f32x4{acc[bb][0], acc[bb][1], acc[bb][2], acc[bb][3]};
          *(f32x4*)&lds[L_YP + lane * 32 + bb * 8 + 4] = f32x4{acc[bb][4], acc[bb][5], acc[bb][6], acc[bb][7]};
        }
        asm volatile("s_waitcnt lgkmcnt(0)" ::: "memory");
        f32x4 g4 = {0.f, 0.f, 0.f, 0.f};
        const float* sp = &lds[L_YP + (c >> 1) * 32 + r * 8 + (c & 1) * 4];
#pragma unroll
        for (int rg = 0; rg < 8; ++rg) g4 += *(const f32x4*)(sp + rg * 256);
        __builtin_amdgcn_raw_buffer_store_b128(v4u{canon(g4[0]), canon(g4[1]), canon(g4[2]), canon(g4[3])}, rw, lane * 16, WS_GU + rho * 1024, AUX_SC1);
      } else {
#pragma unroll
        for (int bb = 0; bb < 4; ++bb) {
          const float f = 1.0f - gate[bb];
          const v4u y0 = {canon(acc[bb][0] * f), canon(acc[bb][1] * f), canon(acc[bb][2] * f), canon(acc[bb][3] * f)};
          const v4u y1 = {canon(acc[bb][4] * f), canon(acc[bb][5] * f), canon(acc[bb][6] * f), canon(acc[bb][7] * f)};
          __builtin_amdgcn_raw_buffer_store_b128(y0, rw, lane * 32, WS_YM + sl * 8192 + bb * 2048, AUX_SC1);
          __builtin_amdgcn_raw_buffer_store_b128(y1, rw, lane * 32 + 16, WS_YM + sl * 8192 + bb * 2048, AUX_SC1);
        }
      }
    }
    STAMP(1);
    lds_wait(&lds[L_SC + 2], NW);
    float M = lds[L_M], L = lds[L_L];
#pragma unroll
    for (int ww = 1; ww < NW; ++ww) { M = fmaxf(M, lds[L_M + ww]); L += lds[L_L + ww]; }
    if (lane == 0) __hip_atomic_store(ML + chunk, ((unsigned long long)canon(L) << 32) | canon(M), RLX_AGENT);
    lds_wait(&lds[L_SC + 3], 6);
    WAIT_GO();
    unsigned long long pm = ~0ull;
#pragma unroll 1
    for (unsigned spin = 0; spin < SPIN_CAP; ++spin) {
      POLL_FENCE();
      pm = __hip_atomic_load(ML + (chunk ^ 1), RLX_AGENT);
      if (__builtin_amdgcn_readfirstlane((unsigned)pm) != SENT) break;
      __builtin_amdgcn_s_sleep(32);
    }
    {
      const float M2 = __uint_as_float((unsigned)pm), L2 = __uint_as_float((unsigned)(pm >> 32));
      const float Mx = fmaxf(M, M2), e1 = __builtin_amdgcn_exp2f(M - Mx), e2 = __builtin_amdgcn_exp2f(M2 - Mx);
      if (lane == 0) lds[L_SC] = gate_b * e1 / (e1 * L + e2 * L2);
    }
    STAMP(2);
    const int u = chunk & 63;
    float xs[8];
#pragma unroll
    for (int k = 0; k < 8; ++k) xs[k] = 0.f;
#pragma unroll 1
    for (int round = 0; round < 2; ++round) {
      const int yo = round ? WS_Y + (b * 64) * 2048 + u * 32 : WS_YM + b * 2048 + u * 32;
      const int vo = round ? lane * 2048 : lane * 8192;
      v4u y0 = {0u, 0u, 0u, 0u}, y1 = y0;
      bool ok = !round && lane >= NSL;
      BAR();
      if (round) { STAMP(3); }
#pragma unroll 1
      for (unsigned spin = 0; spin < SPIN_CAP; ++spin) {
        POLL_FENCE();
        if (!ok) {
          y0 = __builtin_amdgcn_raw_buffer_load_b128(rw, vo, yo, AUX_SC1);
          y1 = __builtin_amdgcn_raw_buffer_load_b128(rw, vo, yo + 16, AUX_SC1);
          ok = y0[0] != SENT && y0[1] != SENT && y0[2] != SENT && y0[3] != SENT && y1[0] != SENT && y1[1] != SENT && y1[2] != SENT && y1[3] != SENT;
        }
        if (__all(ok)) break;
        __builtin_amdgcn_s_sleep(2);
      }
#pragma unroll
      for (int k = 0; k < 4; ++k) { xs[k] += __uint_as_float(y0[k]); xs[4 + k] += __uint_as_float(y1[k]); }
    }
#define TR_STEP(HALF, CTRL, BIT) _Pragma("unroll") for (int hh = 0; hh < (HALF); ++hh) { \
      const float lo_ = xs[hh], hi_ = xs[hh + (HALF)]; const float t1_ = lo_ + dpp<CTRL>(lo_), t2_ = hi_ + dpp<CTRL>(hi_); \
      xs[hh] = (c & (BIT)) ? t2_ : t1_; }
    TR_STEP(4, XOR8, 8) TR_STEP(2, XOR7, 4) TR_STEP(1, XOR2, 2)
    float o = xs[0]; o += dpp<XOR1>(o); o = xrow_sum(o);
    if (lane < 16 && !(lane & 1)) out[b * DM + u * 8 + (lane >> 1)] = __builtin_bit_cast(unsigned short, (__bf16)o);
    STAMP(7);
    return;
  }

  const __amdgpu_buffer_rsrc_t rk = __builtin_amdgcn_make_buffer_rsrc((void*)kc, 0, NB * HQ * SK * DH * 2, RSW3);
  const __amdgpu_buffer_rsrc_t rv = __builtin_amdgcn_make_buffer_rsrc((void*)vc, 0, NB * HQ * SK * DH * 2, RSW3);
  const __amdgpu_buffer_rsrc_t rq = __builtin_amdgcn_make_buffer_rsrc((void*)wq, 0, DM * HQ * DH * 2, RSW3);
  const __amdgpu_buffer_rsrc_t ro = __builtin_amdgcn_make_buffer_rsrc((void*)wo, 0, DM * HQ * DH * 2, RSW3);

  unsigned xd[8];
#pragma unroll
  for (int k = 0; k < 8; ++k) { xd[k] = __builtin_amdgcn_raw_buffer_load_b32(rx, r * 4, (b * DM + 64 * ((k + m8) & 7) + 8 * w) * 2, 0); }
  SB();
  const int kvoff = lane * 16;
  const int kvbase = chunk * (512 * 1024) + w * 4096;
  const int r0 = chunk * 4;
#define KVOFF(t) (kvbase + ((((t) + r0) & 15) << 15))
  v4u wq_[16], wt[16];
  v4u a[4 * RG];
  const int oso = (h * DH + 16 * w) * (DM * 2);
  const int qvoff = r * 2 * (HQ * DH * 2) + c * 16, qso = 8 * w * (HQ * DH * 2) + h * DH * 2;
#define KLOAD(n) { a[n] = __builtin_amdgcn_raw_buffer_load_b128(rk, kvoff, KVOFF((n) >> 2) + ((n) & 3) * 1024, AUX_K); SB(); }
#define LD_O(i) { wt[i] = __builtin_amdgcn_raw_buffer_load_b128(ro, kvoff, oso + (((i) + 2 * m8) & 15) * (DM * 2), AUX_W); SB(); }
#define LD_Q(i) { wq_[i] = __builtin_amdgcn_raw_buffer_load_b128(rq, qvoff, qso + (64 * ((((i) >> 1) + m8) & 7) + ((i) & 1)) * (HQ * DH * 2), AUX_WQ); SB(); }
  LD_Q(0) LD_Q(1) LD_O(0) LD_O(1)
#pragma unroll
  for (int i = 2; i < 16; ++i) LD_Q(i)
  BAR();
  SB();
  STAMP(1);
#pragma unroll
  for (int n = 0; n < K0; ++n) KLOAD(n)
  {
    float qa[8];
#pragma unroll
    for (int k = 0; k < 8; ++k) qa[k] = 0.f;
#pragma unroll
    for (int i = 0; i < 16; ++i) {
      const unsigned xx = xd[i >> 1];
      const unsigned xl = (i & 1) ? (xx >> 16) : (xx & 0xffffu), xh = (i & 1) ? (xx & 0xffff0000u) : (xx << 16);
#pragma unroll
      for (int k = 0; k < 4; ++k) { qa[2 * k] = dot2(wq_[i][k], xl, qa[2 * k]); qa[2 * k + 1] = dot2(wq_[i][k], xh, qa[2 * k + 1]); }
    }
#pragma unroll
    for (int k = 0; k < 8; ++k) qa[k] = xrow_sum(qa[k]);
    KEEP8(qa);
    if (r == 0) {
      *(f32x4*)&lds[L_QP + w * 128 + c * 8] = f32x4{qa[0], qa[1], qa[2], qa[3]};
      *(f32x4*)&lds[L_QP + w * 128 + c * 8 + 4] = f32x4{qa[4], qa[5], qa[6], qa[7]};
    }
  }
  SB();
#pragma unroll
  for (int n = K0; n < 4 * RG; ++n) KLOAD(n)
  BAR();
  STAMP(2);
  unsigned qpk[4];
  {
    f32x4 q0 = {0.f, 0.f, 0.f, 0.f}, q1 = q0;
#pragma unroll
    for (int ww = 0; ww < NW; ++ww) { q0 += *(const f32x4*)&lds[L_QP + ww * 128 + c * 8]; q1 += *(const f32x4*)&lds[L_QP + ww * 128 + c * 8 + 4]; }
    qpk[0] = pk_bf16(q0[0], q0[1]); qpk[1] = pk_bf16(q0[2], q0[3]); qpk[2] = pk_bf16(q1[0], q1[1]); qpk[3] = pk_bf16(q1[2], q1[3]);
    asm volatile("" : "+v"(qpk[0]), "+v"(qpk[1]), "+v"(qpk[2]), "+v"(qpk[3]));
    SB();
  }
  STAMP(3);
  float* prow = &lds[L_P + (w * 4 + r) * 64];
  float mx = -INFINITY;
#define KSTEP(T, RN, AUXN) { \
    float xs[16]; \
    _Pragma("unroll") for (int g = 0; g < 4; ++g) { \
      const int o = KVOFF(4 * (T) + g + RG); \
      _Pragma("unroll") for (int i = 0; i < 4; ++i) { \
        v4u& kk = a[(g % RG) * 4 + i]; \
        float d = dot2(kk[0], qpk[0], 0.f); d = dot2(kk[1], qpk[1], d); d = dot2(kk[2], qpk[2], d); d = dot2(kk[3], qpk[3], d); \
        asm volatile("" : "+v"(d)); \
        xs[g * 4 + i] = d; \
        SB(); \
        kk = __builtin_amdgcn_raw_buffer_load_b128(RN, kvoff, o + i * 1024, AUXN); \
        SB(); \
      } \
    } \
    TR_STEP(8, XOR8, 8) TR_STEP(4, XOR7, 4) TR_STEP(2, XOR2, 2) TR_STEP(1, XOR1, 1) \
    const float sc = xs[0] * (SCALE * LOG2E); \
    prow[16 * (T) + c] = sc; \
    mx = fmaxf(mx, sc); \
    if (tid == 64 * (NW - 1)) __hip_atomic_store((lu32*)&lds[L_SC + 3], (unsigned)(T) + 1u, __ATOMIC_RELAXED, __HIP_MEMORY_SCOPE_WORKGROUP); }
  static_assert(RG == 4, "the last K step refills the whole ring from V");
#pragma unroll 1
  for (int T = 0; T < 3; ++T) KSTEP(T, rk, AUX_K)
  KSTEP(3, rv, AUX_V)
  STAMP(4);
  mx = xrow_max(row_max(mx));
  if (lane == 0) lds[L_M + w] = mx;
  if (lane == 0) lds_arrive(&lds[L_SC + 1]);
  lds_wait(&lds[L_SC + 1], NW);
  {
    const f32x4 m0 = *(const f32x4*)&lds[L_M], m1 = *(const f32x4*)&lds[L_M + 4];
    const float M = fmaxf(fmaxf(fmaxf(m0[0], m0[1]), fmaxf(m0[2], m0[3])), fmaxf(fmaxf(m1[0], m1[1]), fmaxf(m1[2], m1[3])));
    float ls = 0.f;
#pragma unroll
    for (int T = 0; T < 4; ++T) { const float p = __builtin_amdgcn_exp2f(prow[16 * T + c] - M); prow[16 * T + c] = p; ls += p; }
    ls = xrow_sum(row_sum(ls));
    if (lane == 0) lds[L_L + w] = ls;
  }
  SB();
#pragma unroll
  for (int i = 2; i < 16; ++i) LD_O(i)
  if (lane == 0) lds_arrive(&lds[L_SC + 2]);
  lds_wait(&lds[L_SC + 2], NW);
  SB();
  STAMP(5);
  float oa[8];
#pragma unroll
  for (int k = 0; k < 8; ++k) oa[k] = 0.f;
#pragma unroll 1
  for (int T = 0; T < 4; ++T) {
#pragma unroll
    for (int g = 0; g < 4; ++g) {
      const int un = 4 * T + g + RG;
      const int o = un < 16 ? KVOFF(un) : 0x40000000;
      const f32x4 p4 = *(const f32x4*)&prow[16 * T + 4 * g];
#pragma unroll
      for (int i = 0; i < 4; ++i) {
        v4u& vv = a[(g % RG) * 4 + i];
#pragma unroll
        for (int k = 0; k < 4; ++k) { oa[2 * k] = fmaf(p4[i], bflo(vv[k]), oa[2 * k]); oa[2 * k + 1] = fmaf(p4[i], bfhi(vv[k]), oa[2 * k + 1]); }
        KEEP8(oa);
        SB();
        vv = __builtin_amdgcn_raw_buffer_load_b128(rv, kvoff, o + i * 1024, AUX_V);
        SB();
      }
    }
    if (tid == 64 * (NW - 1)) __hip_atomic_store((lu32*)&lds[L_SC + 3], (unsigned)T + 5u, __ATOMIC_RELAXED, __HIP_MEMORY_SCOPE_WORKGROUP);
  }
#pragma unroll
  for (int k = 0; k < 8; ++k) oa[k] = xrow_sum(oa[k]);
  STAMP(6);
  if (r == 0) {
    *(f32x4*)&lds[L_OP + w * 128 + c * 8] = f32x4{oa[0], oa[1], oa[2], oa[3]};
    *(f32x4*)&lds[L_OP + w * 128 + c * 8 + 4] = f32x4{oa[4], oa[5], oa[6], oa[7]};
  }
  BAR();
  float od = 0.f;
#pragma unroll
  for (int ww = 0; ww < NW; ++ww) od += lds[L_OP + ww * 128 + 16 * w + ((c + 2 * m8) & 15)];
  od *= lds[L_SC];
  float ya[8];
#pragma unroll
  for (int k = 0; k < 8; ++k) ya[k] = 0.f;
#pragma unroll
  for (int i = 0; i < 16; ++i) {
    const float ov = __builtin_bit_cast(float, __builtin_amdgcn_readlane(__builtin_bit_cast(int, od), i));
#pragma unroll
    for (int k = 0; k < 4; ++k) { ya[2 * k] = fmaf(ov, bflo(wt[i][k]), ya[2 * k]); ya[2 * k + 1] = fmaf(ov, bfhi(wt[i][k]), ya[2 * k + 1]); }
  }
  *(f32x4*)&lds[L_YP + w * 512 + lane * 8] = f32x4{ya[0], ya[1], ya[2], ya[3]};
  *(f32x4*)&lds[L_YP + w * 512 + lane * 8 + 4] = f32x4{ya[4], ya[5], ya[6], ya[7]};
  BAR();
  if (w < 2) {
    f32x4 y = *(const f32x4*)&lds[L_YP + tid * 4];
#pragma unroll
    for (int ww = 1; ww < NW; ++ww) y += *(const f32x4*)&lds[L_YP + ww * 512 + tid * 4];
    const v4u yu = {canon(y[0]), canon(y[1]), canon(y[2]), canon(y[3])};
    __builtin_amdgcn_raw_buffer_store_b128(yu, rw, tid * 16, WS_Y + chunk * 2048, AUX_SC1);
  }
}
}

extern "C" void kernel_launch(void* const* d_in, const int* in_sizes, int n_in, void* d_out, int out_size, void* d_ws, size_t ws_size,
                              hipStream_t stream) {
  typedef const unsigned short* P;
  hipLaunchKernelGGL(k_main, dim3(NWG), dim3(NTHR), 0, stream, (P)d_in[0], (P)d_in[1], (P)d_in[2], (P)d_in[3], (P)d_in[4], (P)d_in[6], (P)d_in[7],
                     (P)d_in[8], (P)d_in[9], (unsigned short*)d_out, (unsigned char*)d_ws);
}
```

```cpp
#include <hip/hip_runtime.h>
#include <hip/hip_bf16.h>
#include <cstdint>

namespace {
typedef unsigned v4u __attribute__((ext_vector_type(4)));
typedef unsigned v2u __attribute__((ext_vector_type(2)));
typedef float f32x4 __attribute__((ext_vector_type(4)));
typedef __bf16 bf16x2 __attribute__((ext_vector_type(2)));
#define GAS __attribute__((address_space(1)))
typedef GAS unsigned gu32;
typedef GAS unsigned long long gu64;
#define RLX_AGENT __ATOMIC_RELAXED, __HIP_MEMORY_SCOPE_AGENT

constexpr int NB = 4, DM = 512, HQ = 32, SK = 4096, DH = 128, FF = 1376;
constexpr int NWG = 256, NW = 8, NTHR = 64 * (NW + 1);
constexpr int NSL = 43;
constexpr int RSW3 = 0x00020000;
constexpr int AUX_NT = 2, AUX_SC1 = 16;
constexpr float LOG2E = 1.4426950408889634f;
constexpr float SCALE = 0.08838834764831845f;
constexpr unsigned SENT = 0xFFFFFFFFu;
constexpr unsigned SPIN_CAP = 1u << 18;

#define AUX_WQ 2
#define AUX_W 0
#define RG 4
#define AUX_K 2
#define AUX_V 2
#define PRIO_MATH 0
#define K0 8

constexpr int WS_R = 0;
constexpr int WS_E = 4096;
constexpr int WS_CONF = 8192;
constexpr int WS_ML = 12288;
constexpr int WS_Y = 16384;
constexpr int WS_YM = WS_Y + 256 * 2048;
constexpr int WS_GU = WS_YM + NSL * 8192;
constexpr int WS_END = WS_GU + 22 * 4096;

__device__ __forceinline__ float dot2(unsigned a, unsigned b, float acc) {
  return __builtin_amdgcn_fdot2_f32_bf16(__builtin_bit_cast(bf16x2, a), __builtin_bit_cast(bf16x2, b), acc, false);
}
__device__ __forceinline__ float bflo(unsigned u) { return __builtin_bit_cast(float, u << 16); }
__device__ __forceinline__ float bfhi(unsigned u) { return __builtin_bit_cast(float, u & 0xffff0000u); }
__device__ __forceinline__ unsigned pk_bf16(float lo, float hi) {
  bf16x2 r; r.x = (__bf16)lo; r.y = (__bf16)hi; return __builtin_bit_cast(unsigned, r);
}
template <int CTRL> __device__ __forceinline__ float dpp(float x) {
  return __builtin_bit_cast(float, __builtin_amdgcn_mov_dpp(__builtin_bit_cast(int, x), CTRL, 0xf, 0xf, true));
}
constexpr int XOR1 = 0xB1, XOR2 = 0x4E, XOR7 = 0x141, XOR8 = 0x128, ROR4 = 0x124;
__device__ __forceinline__ float xrow_sum(float x) {
  auto s = __builtin_amdgcn_permlane16_swap(__float_as_uint(x), __float_as_uint(x), false, false);
  x = __uint_as_float(s[0]) + __uint_as_float(s[1]);
  auto t = __builtin_amdgcn_permlane32_swap(__float_as_uint(x), __float_as_uint(x), false, false);
  return __uint_as_float(t[0]) + __uint_as_float(t[1]);
}
__device__ __forceinline__ float xrow_max(float x) {
  auto s = __builtin_amdgcn_permlane16_swap(__float_as_uint(x), __float_as_uint(x), false, false);
  x = fmaxf(__uint_as_float(s[0]), __uint_as_float(s[1]));
  auto t = __builtin_amdgcn_permlane32_swap(__float_as_uint(x), __float_as_uint(x), false, false);
  return fmaxf(__uint_as_float(t[0]), __uint_as_float(t[1]));
}
__device__ __forceinline__ float row_sum(float x) { x += dpp<XOR1>(x); x += dpp<XOR2>(x); x += dpp<XOR7>(x); x += dpp<XOR8>(x); return x; }
__device__ __forceinline__ float row_max(float x) { x = fmaxf(x, dpp<XOR1>(x)); x = fmaxf(x, dpp<XOR2>(x)); x = fmaxf(x, dpp<XOR7>(x)); x = fmaxf(x, dpp<XOR8>(x)); return x; }
__device__ __forceinline__ float sigmoidf_(float x) { return 1.0f / (1.0f + __expf(-x)); }
__device__ __forceinline__ unsigned canon(float v) { const unsigned u = __float_as_uint(v); return u == SENT ? 0x7FC00000u : u; }
#define SB() __builtin_amdgcn_sched_barrier(0)
#define POLL_FENCE() asm volatile("" ::: "memory")
#define DRAIN() asm volatile("s_waitcnt vmcnt(0)" ::: "memory")
#define BAR() do { asm volatile("s_waitcnt lgkmcnt(0)" ::: "memory"); __builtin_amdgcn_s_barrier(); asm volatile("" ::: "memory"); } while (0)
#define KEEP8(a) asm volatile("" : "+v"(a[0]), "+v"(a[1]), "+v"(a[2]), "+v"(a[3]), "+v"(a[4]), "+v"(a[5]), "+v"(a[6]), "+v"(a[7]))
#define STAMP(k) do { } while (0)

constexpr int L_QP = 0;
constexpr int L_P = L_QP + 8 * 128;
constexpr int L_M = L_P + 8 * 256;
constexpr int L_L = L_M + 8;
constexpr int L_SC = L_L + 8;
constexpr int L_OP = L_SC + 8;
constexpr int L_YP = L_OP + 8 * 128;
constexpr int L_XT = L_YP + 8 * 512;
constexpr int L_END = L_XT + 272 * 4;
typedef __attribute__((address_space(3))) unsigned lu32;
__device__ __forceinline__ void lds_arrive(float* p) { asm volatile("s_waitcnt lgkmcnt(0)" ::: "memory"); __hip_atomic_fetch_add((lu32*)p, 1u, __ATOMIC_RELAXED, __HIP_MEMORY_SCOPE_WORKGROUP); }
__device__ __forceinline__ void lds_wait(float* p, unsigned n) {
#pragma unroll 1
  for (unsigned spin = 0; spin < (SPIN_CAP << 4); ++spin) {
    if (__builtin_amdgcn_readfirstlane(__hip_atomic_load((lu32*)p, __ATOMIC_RELAXED, __HIP_MEMORY_SCOPE_WORKGROUP)) >= n) break;
    __builtin_amdgcn_s_sleep(1);
  }
  asm volatile("" ::: "memory");
}

__global__ void __launch_bounds__(NTHR, 3)
k_main(const unsigned short* __restrict__ x, const unsigned short* __restrict__ kc, const unsigned short* __restrict__ vc,
       const unsigned short* __restrict__ wq, const unsigned short* __restrict__ wo, const unsigned short* __restrict__ wg,
       const unsigned short* __restrict__ wu, const unsigned short* __restrict__ wd, const unsigned short* __restrict__ gw,
       unsigned short* __restrict__ out, unsigned char* __restrict__ ws) {
  __shared__ __attribute__((aligned(16))) float lds[L_END];
  const int tid = threadIdx.x, lane = tid & 63, w = __builtin_amdgcn_readfirstlane(tid >> 6), r = lane >> 4, c = lane & 15;
  const int bid = blockIdx.x, xg = bid & 7, j = bid >> 3;
  const int h = xg * 4 + (j >> 3), b = (j >> 1) & 3, s = j & 1, m8 = j & 7;
  const int chunk = (b * HQ + h) * 2 + s;
  const __amdgpu_buffer_rsrc_t rx = __builtin_amdgcn_make_buffer_rsrc((void*)x, 0, NB * DM * 2, RSW3);
  const __amdgpu_buffer_rsrc_t rw = __builtin_amdgcn_make_buffer_rsrc((void*)ws, 0, WS_END, RSW3);
  STAMP(0);

  if (w == NW) {
    gu32* R = (gu32*)(ws + WS_R); gu32* E = (gu32*)(ws + WS_E); gu64* ML = (gu64*)(ws + WS_ML);
    __builtin_amdgcn_s_setprio(3);
    if (lane < 3) ((unsigned*)lds)[L_SC + 1 + lane] = 0u;
    const int rho = (bid & 1) ? (bid < 8 ? 127 + (bid >> 1) : 999) : (bid >> 1) - 1;
    const bool gu = (unsigned)rho < 88u, dn = rho >= 88 && rho < 88 + NSL;
    const int sl = rho - 88;
    const unsigned r_old = __hip_atomic_load(R + chunk, RLX_AGENT), e_old = __hip_atomic_load(E + chunk, RLX_AGENT);
    v4u rfirst = __builtin_amdgcn_raw_buffer_load_b128(rw, lane * 16, bid == 0 ? WS_R : 0x40000000, AUX_SC1);
    v4u xv[4];
#pragma unroll
    for (int bb = 0; bb < 4; ++bb) xv[bb] = __builtin_amdgcn_raw_buffer_load_b128(rx, lane * 16, bb * DM * 2, 0);
    const v4u gv = *(const GAS v4u*)((const GAS unsigned char*)gw + lane * 16);
    BAR();
    BAR();
    const v4u sent4 = {SENT, SENT, SENT, SENT};
    __builtin_amdgcn_raw_buffer_store_b128(sent4, rw, lane * 16, WS_Y + chunk * 2048, AUX_SC1);
    __builtin_amdgcn_raw_buffer_store_b128(sent4, rw, lane * 16, WS_Y + chunk * 2048 + 1024, AUX_SC1);
    if (dn) {
#pragma unroll 1
      for (int i = 0; i < 8; ++i) __builtin_amdgcn_raw_buffer_store_b128(sent4, rw, lane * 16, WS_YM + sl * 8192 + i * 1024, AUX_SC1);
    }
    if (gu) __builtin_amdgcn_raw_buffer_store_b128(sent4, rw, lane * 16, WS_GU + rho * 1024, AUX_SC1);
    if (lane == 0) __hip_atomic_store(ML + chunk, ~0ull, RLX_AGENT);
    unsigned nonce = (r_old & ~3u) + 4u; if (e_old - nonce < 4u) nonce += 4u;
    float gate[4];
#pragma unroll
    for (int bb = 0; bb < 4; ++bb) {
      float gd = dot2(xv[bb][0], gv[0], 0.f); gd = dot2(xv[bb][1], gv[1], gd); gd = dot2(xv[bb][2], gv[2], gd); gd = dot2(xv[bb][3], gv[3], gd);
      gate[bb] = sigmoidf_(xrow_sum(row_sum(gd)));
    }
    const float gate_b = b == 0 ? gate[0] : b == 1 ? gate[1] : b == 2 ? gate[2] : gate[3];
#pragma unroll
    for (int k = 0; k < 4; ++k) *(v4u*)&lds[L_XT + (4 * lane + k) * 4] = v4u{xv[0][k], xv[1][k], xv[2][k], xv[3][k]};
    DRAIN();
    if (lane == 0) __hip_atomic_store(R + chunk, nonce, RLX_AGENT);
    const __amdgpu_buffer_rsrc_t rm = __builtin_amdgcn_make_buffer_rsrc((void*)(dn ? wd : (rho & 2) ? wu : wg), 0, (gu || dn) ? DM * FF * 2 : 0, RSW3);
    const int rowb = dn ? DM * 2 : FF * 2;
    const int mvo = dn ? lane * 16 : (lane >> 3) * 2 * (FF * 2) + (lane & 7) * 16;
    const int mso = dn ? sl * 32 * (DM * 2) : (rho & 1) * 256 * (FF * 2) + (rho >> 2) * 128, istep = dn ? 2 * (DM * 2) : 16 * (FF * 2);
    v4u m0[8], m1[8];
    if (gu || dn) {
#pragma unroll
      for (int i = 0; i < 8; ++i) {
        m0[i] = __builtin_amdgcn_raw_buffer_load_b128(rm, mvo, mso + i * istep, AUX_NT); SB();
        m1[i] = __builtin_amdgcn_raw_buffer_load_b128(rm, mvo, mso + i * istep + rowb, AUX_NT); SB();
      }
    }
    if (bid == 0) {
      unsigned seen = 0;
#pragma unroll 1
      for (unsigned spin = 0; spin < SPIN_CAP; ++spin) {
        POLL_FENCE();
        __builtin_amdgcn_s_sleep(40);
        v4u rq = __builtin_amdgcn_raw_buffer_load_b128(rw, lane * 16, WS_R, AUX_SC1);
#pragma unroll
        for (int k = 0; k < 4; ++k) seen |= (rq[k] != rfirst[k]) ? 1u << k : 0u;
        if (__all(seen == 15u)) {
          __builtin_amdgcn_raw_buffer_store_b128((rq & ~3u) + 2u, rw, lane * 16, WS_E, AUX_SC1);
          break;
        }
        if (spin >= 2) {
#pragma unroll
          for (int k = 0; k < 4; ++k) if (!(seen >> k & 1u)) __hip_atomic_store(E + lane * 4 + k, rq[k], RLX_AGENT);
        }
      }
    }
    STAMP(4);
#define WAIT_GO() do { _Pragma("unroll 1") for (unsigned spin = 0; spin < SPIN_CAP; ++spin) { POLL_FENCE(); \
        const unsigned e_ = __builtin_amdgcn_readfirstlane(__hip_atomic_load(E + chunk, RLX_AGENT)); \
        if (e_ == nonce + 2u) break; \
        if (e_ == nonce && lane == 0) __hip_atomic_store(R + chunk, nonce + 1u, RLX_AGENT); \
        __builtin_amdgcn_s_sleep(32); } } while (0)
    if (gu || dn) {
      if (dn) {
        lds_wait(&lds[L_SC + 3], 3);
        WAIT_GO();
        v2u pv[4];
        const int go = WS_GU + (sl >> 1) * 4096 + (sl & 1) * 128, gvo = r * 256 + c * 8;
#pragma unroll 1
        for (unsigned spin = 0; spin < SPIN_CAP; ++spin) {
          POLL_FENCE();
          bool ok = true;
#pragma unroll
          for (int k = 0; k < 4; ++k) { pv[k] = __builtin_amdgcn_raw_buffer_load_b64(rw, gvo, go + k * 1024, AUX_SC1); }
#pragma unroll
          for (int k = 0; k < 4; ++k) ok = ok && pv[k][0] != SENT && pv[k][1] != SENT;
          if (__all(ok)) break;
          __builtin_amdgcn_s_sleep(32);
        }
        const float g0 = __uint_as_float(pv[0][0]) + __uint_as_float(pv[1][0]), g1 = __uint_as_float(pv[0][1]) + __uint_as_float(pv[1][1]);
        const float u0 = __uint_as_float(pv[2][0]) + __uint_as_float(pv[3][0]), u1 = __uint_as_float(pv[2][1]) + __uint_as_float(pv[3][1]);
        ((unsigned*)lds)[L_XT + (256 + c) * 4 + r] = pk_bf16(sigmoidf_(g0) * u0, sigmoidf_(g1) * u1);
        asm volatile("s_waitcnt lgkmcnt(0)" ::: "memory");
      }
      const float* xt = &lds[L_XT + (dn ? 256 * 4 : ((rho & 1) * 128 + (lane >> 3)) * 4)];
      const int xstep = dn ? 4 : 32;
      float acc[4][8];
#pragma unroll
      for (int bb = 0; bb < 4; ++bb)
#pragma unroll
        for (int k = 0; k < 8; ++k) acc[bb][k] = 0.f;
#pragma unroll 1
      for (int I0 = 0; I0 < 16; I0 += 8) {
#pragma unroll
        for (int i = 0; i < 8; ++i) {
          __builtin_amdgcn_s_setprio(PRIO_MATH);
          const v4u xp = *(const v4u*)(xt + (I0 + i) * xstep);
#pragma unroll
          for (int k = 0; k < 4; ++k) {
            const unsigned lo = __builtin_amdgcn_perm(m1[i][k], m0[i][k], 0x05040100u), hi = __builtin_amdgcn_perm(m1[i][k], m0[i][k], 0x07060302u);
#pragma unroll
            for (int bb = 0; bb < 4; ++bb) { acc[bb][2 * k] = dot2(lo, xp[bb], acc[bb][2 * k]); acc[bb][2 * k + 1] = dot2(hi, xp[bb], acc[bb][2 * k + 1]); }
          }
          KEEP8(acc[0]); KEEP8(acc[1]); KEEP8(acc[2]); KEEP8(acc[3]);
          SB();
          __builtin_amdgcn_s_setprio(3);
          const int o = I0 == 0 ? mso + (8 + i) * istep : 0x40000000;
          m0[i] = __builtin_amdgcn_raw_buffer_load_b128(rm, mvo, o, AUX_NT); SB();
          m1[i] = __builtin_amdgcn_raw_buffer_load_b128(rm, mvo, o + rowb, AUX_NT); SB();
        }
      }
      if (!dn) {
#pragma unroll
        for (int bb = 0; bb < 4; ++bb) {
          *(f32x4*)&lds[L_YP + lane * 32 + bb * 8] = f32x4{acc[bb][0], acc[bb][1], acc[bb][2], acc[bb][3]};
          *(f32x4*)&lds[L_YP + lane * 32 + bb * 8 + 4] = f32x4{acc[bb][4], acc[bb][5], acc[bb][6], acc[bb][7]};
        }
        asm volatile("s_waitcnt lgkmcnt(0)" ::: "memory");
        f32x4 g4 = {0.f, 0.f, 0.f, 0.f};
        const float* sp = &lds[L_YP + (c >> 1) * 32 + r * 8 + (c & 1) * 4];
#pragma unroll
        for (int rg = 0; rg < 8; ++rg) g4 += *(const f32x4*)(sp + rg * 256);
        __builtin_amdgcn_raw_buffer_store_b128(v4u{canon(g4[0]), canon(g4[1]), canon(g4[2]), canon(g4[3])}, rw, lane * 16, WS_GU + rho * 1024, AUX_SC1);
      } else {
#pragma unroll
        for (int bb = 0; bb < 4; ++bb) {
          const float f = 1.0f - gate[bb];
          const v4u y0 = {canon(acc[bb][0] * f), canon(acc[bb][1] * f), canon(acc[bb][2] * f), canon(acc[bb][3] * f)};
          const v4u y1 = {canon(acc[bb][4] * f), canon(acc[bb][5] * f), canon(acc[bb][6] * f), canon(acc[bb][7] * f)};
          __builtin_amdgcn_raw_buffer_store_b128(y0, rw, lane * 32, WS_YM + sl * 8192 + bb * 2048, AUX_SC1);
          __builtin_amdgcn_raw_buffer_store_b128(y1, rw, lane * 32 + 16, WS_YM + sl * 8192 + bb * 2048, AUX_SC1);
        }
      }
    }
    STAMP(1);
    lds_wait(&lds[L_SC + 2], NW);
    float M = lds[L_M], L = lds[L_L];
#pragma unroll
    for (int ww = 1; ww < NW; ++ww) { M = fmaxf(M, lds[L_M + ww]); L += lds[L_L + ww]; }
    if (lane == 0) __hip_atomic_store(ML + chunk, ((unsigned long long)canon(L) << 32) | canon(M), RLX_AGENT);
    if (!dn) WAIT_GO();
    lds_wait(&lds[L_SC + 3], 6);
    unsigned long long pm = ~0ull;
#pragma unroll 1
    for (unsigned spin = 0; spin < SPIN_CAP; ++spin) {
      POLL_FENCE();
      pm = __hip_atomic_load(ML + (chunk ^ 1), RLX_AGENT);
      if (__builtin_amdgcn_readfirstlane((unsigned)pm) != SENT) break;
      __builtin_amdgcn_s_sleep(32);
    }
    {
      const float M2 = __uint_as_float((unsigned)pm), L2 = __uint_as_float((unsigned)(pm >> 32));
      const float Mx = fmaxf(M, M2), e1 = __builtin_amdgcn_exp2f(M - Mx), e2 = __builtin_amdgcn_exp2f(M2 - Mx);
      if (lane == 0) lds[L_SC] = gate_b * e1 / (e1 * L + e2 * L2);
    }
    STAMP(2);
    const int u = chunk & 63;
    float xs[8];
#pragma unroll
    for (int k = 0; k < 8; ++k) xs[k] = 0.f;
#pragma unroll 1
    for (int round = 0; round < 2; ++round) {
      const int yo = round ? WS_Y + (b * 64) * 2048 + u * 32 : WS_YM + b * 2048 + u * 32;
      const int vo = round ? lane * 2048 : lane * 8192;
      v4u y0 = {0u, 0u, 0u, 0u}, y1 = y0;
      bool ok = !round && lane >= NSL;
      BAR();
      if (round) { STAMP(3); }
#pragma unroll 1
      for (unsigned spin = 0; spin < SPIN_CAP; ++spin) {
        POLL_FENCE();
        if (!ok) {
          y0 = __builtin_amdgcn_raw_buffer_load_b128(rw, vo, yo, AUX_SC1);
          y1 = __builtin_amdgcn_raw_buffer_load_b128(rw, vo, yo + 16, AUX_SC1);
          ok = y0[0] != SENT && y0[1] != SENT && y0[2] != SENT && y0[3] != SENT && y1[0] != SENT && y1[1] != SENT && y1[2] != SENT && y1[3] != SENT;
        }
        if (__all(ok)) break;
        __builtin_amdgcn_s_sleep(2);
      }
#pragma unroll
      for (int k = 0; k < 4; ++k) { xs[k] += __uint_as_float(y0[k]); xs[4 + k] += __uint_as_float(y1[k]); }
    }
#define TR_STEP(HALF, CTRL, BIT) _Pragma("unroll") for (int hh = 0; hh < (HALF); ++hh) { \
      const float lo_ = xs[hh], hi_ = xs[hh + (HALF)]; const float t1_ = lo_ + dpp<CTRL>(lo_), t2_ = hi_ + dpp<CTRL>(hi_); \
      xs[hh] = (c & (BIT)) ? t2_ : t1_; }
    TR_STEP(4, XOR8, 8) TR_STEP(2, XOR7, 4) TR_STEP(1, XOR2, 2)
    float o = xs[0]; o += dpp<XOR1>(o); o = xrow_sum(o);
    if (lane < 16 && !(lane & 1)) out[b * DM + u * 8 + (lane >> 1)] = __builtin_bit_cast(unsigned short, (__bf16)o);
    STAMP(7);
    return;
  }

  const __amdgpu_buffer_rsrc_t rk = __builtin_amdgcn_make_buffer_rsrc((void*)kc, 0, NB * HQ * SK * DH * 2, RSW3);
  const __amdgpu_buffer_rsrc_t rv = __builtin_amdgcn_make_buffer_rsrc((void*)vc, 0, NB * HQ * SK * DH * 2, RSW3);
  const __amdgpu_buffer_rsrc_t rq = __builtin_amdgcn_make_buffer_rsrc((void*)wq, 0, DM * HQ * DH * 2, RSW3);
  const __amdgpu_buffer_rsrc_t ro = __builtin_amdgcn_make_buffer_rsrc((void*)wo, 0, DM * HQ * DH * 2, RSW3);

  unsigned xd[8];
#pragma unroll
  for (int k = 0; k < 8; ++k) { xd[k] = __builtin_amdgcn_raw_buffer_load_b32(rx, r * 4, (b * DM + 64 * ((k + m8) & 7) + 8 * w) * 2, 0); }
  SB();
  const int kvoff = lane * 16;
  const int kvbase = chunk * (512 * 1024) + w * 4096;
  const int r0 = chunk * 4;
#define KVOFF(t) (kvbase + ((((t) + r0) & 15) << 15))
  v4u wq_[16], wt[16];
  v4u a[4 * RG];
  const int oso = (h * DH + 16 * w) * (DM * 2);
  const int qvoff = r * 2 * (HQ * DH * 2) + c * 16, qso = 8 * w * (HQ * DH * 2) + h * DH * 2;
#define KLOAD(n) { a[n] = __builtin_amdgcn_raw_buffer_load_b128(rk, kvoff, KVOFF((n) >> 2) + ((n) & 3) * 1024, AUX_K); SB(); }
#define LD_O(i) { wt[i] = __builtin_amdgcn_raw_buffer_load_b128(ro, kvoff, oso + (((i) + 2 * m8) & 15) * (DM * 2), AUX_W); SB(); }
#define LD_Q(i) { wq_[i] = __builtin_amdgcn_raw_buffer_load_b128(rq, qvoff, qso + (64 * ((((i) >> 1) + m8) & 7) + ((i) & 1)) * (HQ * DH * 2), AUX_WQ); SB(); }
  LD_Q(0) LD_Q(1) LD_O(0) LD_O(1)
#pragma unroll
  for (int i = 2; i < 16; ++i) LD_Q(i)
  BAR();
  SB();
  STAMP(1);
#pragma unroll
  for (int n = 0; n < K0; ++n) KLOAD(n)
  {
    float qa[8];
#pragma unroll
    for (int k = 0; k < 8; ++k) qa[k] = 0.f;
#pragma unroll
    for (int i = 0; i < 16; ++i) {
      const unsigned xx = xd[i >> 1];
      const unsigned xl = (i & 1) ? (xx >> 16) : (xx & 0xffffu), xh = (i & 1) ? (xx & 0xffff0000u) : (xx << 16);
#pragma unroll
      for (int k = 0; k < 4; ++k) { qa[2 * k] = dot2(wq_[i][k], xl, qa[2 * k]); qa[2 * k + 1] = dot2(wq_[i][k], xh, qa[2 * k + 1]); }
    }
#pragma unroll
    for (int k = 0; k < 8; ++k) qa[k] = xrow_sum(qa[k]);
    KEEP8(qa);
    if (r == 0) {
      *(f32x4*)&lds[L_QP + w * 128 + c * 8] = f32x4{qa[0], qa[1], qa[2], qa[3]};
      *(f32x4*)&lds[L_QP + w * 128 + c * 8 + 4] = f32x4{qa[4], qa[5], qa[6], qa[7]};
    }
  }
  SB();
#pragma unroll
  for (int n = K0; n < 4 * RG; ++n) KLOAD(n)
  BAR();
  STAMP(2);
  unsigned qpk[4];
  {
    f32x4 q0 = {0.f, 0.f, 0.f, 0.f}, q1 = q0;
#pragma unroll
    for (int ww = 0; ww < NW; ++ww) { q0 += *(const f32x4*)&lds[L_QP + ww * 128 + c * 8]; q1 += *(const f32x4*)&lds[L_QP + ww * 128 + c * 8 + 4]; }
    qpk[0] = pk_bf16(q0[0], q0[1]); qpk[1] = pk_bf16(q0[2], q0[3]); qpk[2] = pk_bf16(q1[0], q1[1]); qpk[3] = pk_bf16(q1[2], q1[3]);
    asm volatile("" : "+v"(qpk[0]), "+v"(qpk[1]), "+v"(qpk[2]), "+v"(qpk[3]));
    SB();
  }
  STAMP(3);
  float* prow = &lds[L_P + (w * 4 + r) * 64];
  float mx = -INFINITY;
#define KSTEP(T, RN, AUXN) { \
    float xs[16]; \
    _Pragma("unroll") for (int g = 0; g < 4; ++g) { \
      const int o = KVOFF(4 * (T) + g + RG); \
      _Pragma("unroll") for (int i = 0; i < 4; ++i) { \
        v4u& kk = a[(g % RG) * 4 + i]; \
        float d = dot2(kk[0], qpk[0], 0.f); d = dot2(kk[1], qpk[1], d); d = dot2(kk[2], qpk[2], d); d = dot2(kk[3], qpk[3], d); \
        asm volatile("" : "+v"(d)); \
        xs[g * 4 + i] = d; \
        SB(); \
        kk = __builtin_amdgcn_raw_buffer_load_b128(RN, kvoff, o + i * 1024, AUXN); \
        SB(); \
      } \
    } \
    TR_STEP(8, XOR8, 8) TR_STEP(4, XOR7, 4) TR_STEP(2, XOR2, 2) TR_STEP(1, XOR1, 1) \
    const float sc = xs[0] * (SCALE * LOG2E); \
    prow[16 * (T) + c] = sc; \
    mx = fmaxf(mx, sc); \
    if (tid == 64 * (NW - 1)) __hip_atomic_store((lu32*)&lds[L_SC + 3], (unsigned)(T) + 1u, __ATOMIC_RELAXED, __HIP_MEMORY_SCOPE_WORKGROUP); }
  static_assert(RG == 4, "the last K step refills the whole ring from V");
#pragma unroll 1
  for (int T = 0; T < 3; ++T) KSTEP(T, rk, AUX_K)
  KSTEP(3, rv, AUX_V)
  STAMP(4);
  mx = xrow_max(row_max(mx));
  if (lane == 0) lds[L_M + w] = mx;
  if (lane == 0) lds_arrive(&lds[L_SC + 1]);
  lds_wait(&lds[L_SC + 1], NW);
  {
    const f32x4 m0 = *(const f32x4*)&lds[L_M], m1 = *(const f32x4*)&lds[L_M + 4];
    const float M = fmaxf(fmaxf(fmaxf(m0[0], m0[1]), fmaxf(m0[2], m0[3])), fmaxf(fmaxf(m1[0], m1[1]), fmaxf(m1[2], m1[3])));
    float ls = 0.f;
#pragma unroll
    for (int T = 0; T < 4; ++T) { const float p = __builtin_amdgcn_exp2f(prow[16 * T + c] - M); prow[16 * T + c] = p; ls += p; }
    ls = xrow_sum(row_sum(ls));
    if (lane == 0) lds[L_L + w] = ls;
  }
  SB();
#pragma unroll
  for (int i = 2; i < 16; ++i) LD_O(i)
  if (lane == 0) lds_arrive(&lds[L_SC + 2]);
  lds_wait(&lds[L_SC + 2], NW);
  SB();
  STAMP(5);
  float oa[8];
#pragma unroll
  for (int k = 0; k < 8; ++k) oa[k] = 0.f;
#pragma unroll 1
  for (int T = 0; T < 4; ++T) {
#pragma unroll
    for (int g = 0; g < 4; ++g) {
      const int un = 4 * T + g + RG;
      const int o = un < 16 ? KVOFF(un) : 0x40000000;
      const f32x4 p4 = *(const f32x4*)&prow[16 * T + 4 * g];
#pragma unroll
      for (int i = 0; i < 4; ++i) {
        v4u& vv = a[(g % RG) * 4 + i];
#pragma unroll
        for (int k = 0; k < 4; ++k) { oa[2 * k] = fmaf(p4[i], bflo(vv[k]), oa[2 * k]); oa[2 * k + 1] = fmaf(p4[i], bfhi(vv[k]), oa[2 * k + 1]); }
        KEEP8(oa);
        SB();
        vv = __builtin_amdgcn_raw_buffer_load_b128(rv, kvoff, o + i * 1024, AUX_V);
        SB();
      }
    }
    if (tid == 64 * (NW - 1)) __hip_atomic_store((lu32*)&lds[L_SC + 3], (unsigned)T + 5u, __ATOMIC_RELAXED, __HIP_MEMORY_SCOPE_WORKGROUP);
  }
#pragma unroll
  for (int k = 0; k < 8; ++k) oa[k] = xrow_sum(oa[k]);
  STAMP(6);
  if (r == 0) {
    *(f32x4*)&lds[L_OP + w * 128 + c * 8] = f32x4{oa[0], oa[1], oa[2], oa[3]};
    *(f32x4*)&lds[L_OP + w * 128 + c * 8 + 4] = f32x4{oa[4], oa[5], oa[6], oa[7]};
  }
  BAR();
  float od = 0.f;
#pragma unroll
  for (int ww = 0; ww < NW; ++ww) od += lds[L_OP + ww * 128 + 16 * w + ((c + 2 * m8) & 15)];
  od *= lds[L_SC];
  float ya[8];
#pragma unroll
  for (int k = 0; k < 8; ++k) ya[k] = 0.f;
#pragma unroll
  for (int i = 0; i < 16; ++i) {
    const float ov = __builtin_bit_cast(float, __builtin_amdgcn_readlane(__builtin_bit_cast(int, od), i));
#pragma unroll
    for (int k = 0; k < 4; ++k) { ya[2 * k] = fmaf(ov, bflo(wt[i][k]), ya[2 * k]); ya[2 * k + 1] = fmaf(ov, bfhi(wt[i][k]), ya[2 * k + 1]); }
  }
  *(f32x4*)&lds[L_YP + w * 512 + lane * 8] = f32x4{ya[0], ya[1], ya[2], ya[3]};
  *(f32x4*)&lds[L_YP + w * 512 + lane * 8 + 4] = f32x4{ya[4], ya[5], ya[6], ya[7]};
  BAR();
  if (w < 2) {
    f32x4 y = *(const f32x4*)&lds[L_YP + tid * 4];
#pragma unroll
    for (int ww = 1; ww < NW; ++ww) y += *(const f32x4*)&lds[L_YP + ww * 512 + tid * 4];
    const v4u yu = {canon(y[0]), canon(y[1]), canon(y[2]), canon(y[3])};
    __builtin_amdgcn_raw_buffer_store_b128(yu, rw, tid * 16, WS_Y + chunk * 2048, AUX_SC1);
  }
}
}

extern "C" void kernel_launch(void* const* d_in, const int* in_sizes, int n_in, void* d_out, int out_size, void* d_ws, size_t ws_size,
                              hipStream_t stream) {
  typedef const unsigned short* P;
  hipLaunchKernelGGL(k_main, dim3(NWG), dim3(NTHR), 0, stream, (P)d_in[0], (P)d_in[1], (P)d_in[2], (P)d_in[3], (P)d_in[4], (P)d_in[6], (P)d_in[7],
                     (P)d_in[8], (P)d_in[9], (unsigned short*)d_out, (unsigned char*)d_ws);
}
```

```cpp
#include <hip/hip_runtime.h>
#include <hip/hip_bf16.h>
#include <cstdint>

namespace {
typedef unsigned v4u __attribute__((ext_vector_type(4)));
typedef unsigned v2u __attribute__((ext_vector_type(2)));
typedef float f32x4 __attribute__((ext_vector_type(4)));
typedef __bf16 bf16x2 __attribute__((ext_vector_type(2)));
#define GAS __attribute__((address_space(1)))
typedef GAS unsigned gu32;
typedef GAS unsigned long long gu64;
#define RLX_AGENT __ATOMIC_RELAXED, __HIP_MEMORY_SCOPE_AGENT

constexpr int NB = 4, DM = 512, HQ = 32, SK = 4096, DH = 128, FF = 1376;
constexpr int NWG = 256, NW = 8, NTHR = 64 * (NW + 1);
constexpr int NSL = 43;
constexpr int RSW3 = 0x00020000;
constexpr int AUX_NT = 2, AUX_SC1 = 16;
constexpr float LOG2E = 1.4426950408889634f;
constexpr float SCALE = 0.08838834764831845f;
constexpr unsigned SENT = 0xFFFFFFFFu;
constexpr unsigned SPIN_CAP = 1u << 18;

#define AUX_WQ 0
#define AUX_W 0
#define RG 4
#define AUX_K 2
#define AUX_V 2
#define PRIO_MATH 0
#define PACE 0
#define STAGGER 1
#define NOMLP 0
#define ZQ 0
#define ZO 0
#define K0 4

constexpr int WS_R = 0;
constexpr int WS_E = 4096;
constexpr int WS_CONF = 8192;
constexpr int WS_ML = 12288;
constexpr int WS_Y = 16384;
constexpr int WS_YM = WS_Y + 256 * 2048;
constexpr int WS_GU = WS_YM + NSL * 8192;
constexpr int WS_END = WS_GU + 22 * 4096;

__device__ __forceinline__ float dot2(unsigned a, unsigned b, float acc) {
  return __builtin_amdgcn_fdot2_f32_bf16(__builtin_bit_cast(bf16x2, a), __builtin_bit_cast(bf16x2, b), acc, false);
}
__device__ __forceinline__ float bflo(unsigned u) { return __builtin_bit_cast(float, u << 16); }
__device__ __forceinline__ float bfhi(unsigned u) { return __builtin_bit_cast(float, u & 0xffff0000u); }
__device__ __forceinline__ unsigned pk_bf16(float lo, float hi) {
  bf16x2 r; r.x = (__bf16)lo; r.y = (__bf16)hi; return __builtin_bit_cast(unsigned, r);
}
template <int CTRL> __device__ __forceinline__ float dpp(float x) {
  return __builtin_bit_cast(float, __builtin_amdgcn_mov_dpp(__builtin_bit_cast(int, x), CTRL, 0xf, 0xf, true));
}
constexpr int XOR1 = 0xB1, XOR2 = 0x4E, XOR7 = 0x141, XOR8 = 0x128, ROR4 = 0x124;
__device__ __forceinline__ float xrow_sum(float x) {
  auto s = __builtin_amdgcn_permlane16_swap(__float_as_uint(x), __float_as_uint(x), false, false);
  x = __uint_as_float(s[0]) + __uint_as_float(s[1]);
  auto t = __builtin_amdgcn_permlane32_swap(__float_as_uint(x), __float_as_uint(x), false, false);
  return __uint_as_float(t[0]) + __uint_as_float(t[1]);
}
__device__ __forceinline__ float xrow_max(float x) {
  auto s = __builtin_amdgcn_permlane16_swap(__float_as_uint(x), __float_as_uint(x), false, false);
  x = fmaxf(__uint_as_float(s[0]), __uint_as_float(s[1]));
  auto t = __builtin_amdgcn_permlane32_swap(__float_as_uint(x), __float_as_uint(x), false, false);
  return fmaxf(__uint_as_float(t[0]), __uint_as_float(t[1]));
}
__device__ __forceinline__ float row_sum(float x) { x += dpp<XOR1>(x); x += dpp<XOR2>(x); x += dpp<XOR7>(x); x += dpp<XOR8>(x); return x; }
__device__ __forceinline__ float row_max(float x) { x = fmaxf(x, dpp<XOR1>(x)); x = fmaxf(x, dpp<XOR2>(x)); x = fmaxf(x, dpp<XOR7>(x)); x = fmaxf(x, dpp<XOR8>(x)); return x; }
__device__ __forceinline__ float sigmoidf_(float x) { return 1.0f / (1.0f + __expf(-x)); }
__device__ __forceinline__ unsigned canon(float v) { const unsigned u = __float_as_uint(v); return u == SENT ? 0x7FC00000u : u; }
#define SB() __builtin_amdgcn_sched_barrier(0)
#define POLL_FENCE() asm volatile("" ::: "memory")
#define DRAIN() asm volatile("s_waitcnt vmcnt(0)" ::: "memory")
#define BAR() do { asm volatile("s_waitcnt lgkmcnt(0)" ::: "memory"); __builtin_amdgcn_s_barrier(); asm volatile("" ::: "memory"); } while (0)
#define KEEP8(a) asm volatile("" : "+v"(a[0]), "+v"(a[1]), "+v"(a[2]), "+v"(a[3]), "+v"(a[4]), "+v"(a[5]), "+v"(a[6]), "+v"(a[7]))
#define STAMP(k) do { } while (0)

constexpr int L_QP = 0;
constexpr int L_P = L_QP + 8 * 128;
constexpr int L_M = L_P + 8 * 256;
constexpr int L_L = L_M + 8;
constexpr int L_SC = L_L + 8;
constexpr int L_F = L_SC + 8;
constexpr int L_OP = L_F + 8;
constexpr int L_YP = L_OP + 8 * 128;
constexpr int L_XT = L_YP + 8 * 512;
constexpr int L_END = L_XT + 272 * 4;
typedef __attribute__((address_space(3))) unsigned lu32;
__device__ __forceinline__ void lds_arrive(float* p) { asm volatile("s_waitcnt lgkmcnt(0)" ::: "memory"); __hip_atomic_fetch_add((lu32*)p, 1u, __ATOMIC_RELAXED, __HIP_MEMORY_SCOPE_WORKGROUP); }
__device__ __forceinline__ void lds_wait(float* p, unsigned n) {
#pragma unroll 1
  for (unsigned spin = 0; spin < (SPIN_CAP << 4); ++spin) {
    if (__builtin_amdgcn_readfirstlane(__hip_atomic_load((lu32*)p, __ATOMIC_RELAXED, __HIP_MEMORY_SCOPE_WORKGROUP)) >= n) break;
    __builtin_amdgcn_s_sleep(1);
  }
  asm volatile("" ::: "memory");
}

__global__ void __launch_bounds__(NTHR, 3)
k_main(const unsigned short* __restrict__ x, const unsigned short* __restrict__ kc, const unsigned short* __restrict__ vc,
       const unsigned short* __restrict__ wq, const unsigned short* __restrict__ wo, const unsigned short* __restrict__ wg,
       const unsigned short* __restrict__ wu, const unsigned short* __restrict__ wd, const unsigned short* __restrict__ gw,
       unsigned short* __restrict__ out, unsigned char* __restrict__ ws) {
  __shared__ __attribute__((aligned(16))) float lds[L_END];
  const int tid = threadIdx.x, lane = tid & 63, w = __builtin_amdgcn_readfirstlane(tid >> 6), r = lane >> 4, c = lane & 15;
  const int bid = blockIdx.x, xg = bid & 7, j = bid >> 3;
  const int h = xg * 4 + (j >> 3), b = (j >> 1) & 3, s = j & 1, m8 = j & 7;
  const int chunk = (b * HQ + h) * 2 + s;
  const __amdgpu_buffer_rsrc_t rx = __builtin_amdgcn_make_buffer_rsrc((void*)x, 0, NB * DM * 2, RSW3);
  const __amdgpu_buffer_rsrc_t rw = __builtin_amdgcn_make_buffer_rsrc((void*)ws, 0, WS_END, RSW3);
  STAMP(0);

  if (w == NW) {
    gu32* R = (gu32*)(ws + WS_R); gu32* E = (gu32*)(ws + WS_E); gu64* ML = (gu64*)(ws + WS_ML);
    __builtin_amdgcn_s_setprio(3);
    if (lane < 3) ((unsigned*)lds)[L_SC + 1 + lane] = 0u;
    const int rho = NOMLP ? 999 : (bid & 1) ? (bid < 8 ? 127 + (bid >> 1) : 999) : (bid >> 1) - 1;
    const bool gu = (unsigned)rho < 88u, dn = rho >= 88 && rho < 88 + NSL;
    const int sl = rho - 88;
    const unsigned r_old = __hip_atomic_load(R + chunk, RLX_AGENT), e_old = __hip_atomic_load(E + chunk, RLX_AGENT);
    v4u rfirst = __builtin_amdgcn_raw_buffer_load_b128(rw, lane * 16, bid == 0 ? WS_R : 0x40000000, AUX_SC1);
    v4u xv[4];
#pragma unroll
    for (int bb = 0; bb < 4; ++bb) xv[bb] = __builtin_amdgcn_raw_buffer_load_b128(rx, lane * 16, bb * DM * 2, 0);
    const v4u gv = *(const GAS v4u*)((const GAS unsigned char*)gw + lane * 16);
    BAR();
    BAR();
    const v4u sent4 = {SENT, SENT, SENT, SENT};
    __builtin_amdgcn_raw_buffer_store_b128(sent4, rw, lane * 16, WS_Y + chunk * 2048, AUX_SC1);
    __builtin_amdgcn_raw_buffer_store_b128(sent4, rw, lane * 16, WS_Y + chunk * 2048 + 1024, AUX_SC1);
    if (dn) {
#pragma unroll 1
      for (int i = 0; i < 8; ++i) __builtin_amdgcn_raw_buffer_store_b128(sent4, rw, lane * 16, WS_YM + sl * 8192 + i * 1024, AUX_SC1);
    }
    if (gu) __builtin_amdgcn_raw_buffer_store_b128(sent4, rw, lane * 16, WS_GU + rho * 1024, AUX_SC1);
    if (lane == 0) __hip_atomic_store(ML + chunk, ~0ull, RLX_AGENT);
    unsigned nonce = (r_old & ~3u) + 4u; if (e_old - nonce < 4u) nonce += 4u;
    float gate[4];
#pragma unroll
    for (int bb = 0; bb < 4; ++bb) {
      float gd = dot2(xv[bb][0], gv[0], 0.f); gd = dot2(xv[bb][1], gv[1], gd); gd = dot2(xv[bb][2], gv[2], gd); gd = dot2(xv[bb][3], gv[3], gd);
      gate[bb] = sigmoidf_(xrow_sum(row_sum(gd)));
    }
    const float gate_b = b == 0 ? gate[0] : b == 1 ? gate[1] : b == 2 ? gate[2] : gate[3];
#pragma unroll
    for (int k = 0; k < 4; ++k) *(v4u*)&lds[L_XT + (4 * lane + k) * 4] = v4u{xv[0][k], xv[1][k], xv[2][k], xv[3][k]};
    DRAIN();
    if (lane == 0) __hip_atomic_store(R + chunk, nonce, RLX_AGENT);
    const __amdgpu_buffer_rsrc_t rm = __builtin_amdgcn_make_buffer_rsrc((void*)(dn ? wd : (rho & 2) ? wu : wg), 0, (gu || dn) ? DM * FF * 2 : 0, RSW3);
    const int rowb = dn ? DM * 2 : FF * 2;
    const int mvo = dn ? lane * 16 : (lane >> 3) * 2 * (FF * 2) + (lane & 7) * 16;
    const int mso = dn ? sl * 32 * (DM * 2) : (rho & 1) * 256 * (FF * 2) + (rho >> 2) * 128, istep = dn ? 2 * (DM * 2) : 16 * (FF * 2);
    v4u m0[8], m1[8];
    if (gu || dn) {
#pragma unroll
      for (int i = 0; i < 8; ++i) {
        m0[i] = __builtin_amdgcn_raw_buffer_load_b128(rm, mvo, mso + i * istep, AUX_NT); SB();
        m1[i] = __builtin_amdgcn_raw_buffer_load_b128(rm, mvo, mso + i * istep + rowb, AUX_NT); SB();
      }
    }
    if (bid == 0) {
      unsigned seen = 0;
#pragma unroll 1
      for (unsigned spin = 0; spin < SPIN_CAP; ++spin) {
        POLL_FENCE();
        __builtin_amdgcn_s_sleep(40);
        v4u rq = __builtin_amdgcn_raw_buffer_load_b128(rw, lane * 16, WS_R, AUX_SC1);
#pragma unroll
        for (int k = 0; k < 4; ++k) seen |= (rq[k] != rfirst[k]) ? 1u << k : 0u;
        if (__all(seen == 15u)) {
          __builtin_amdgcn_raw_buffer_store_b128((rq & ~3u) + 2u, rw, lane * 16, WS_E, AUX_SC1);
          break;
        }
        if (spin >= 2) {
#pragma unroll
          for (int k = 0; k < 4; ++k) if (!(seen >> k & 1u)) __hip_atomic_store(E + lane * 4 + k, rq[k], RLX_AGENT);
        }
      }
    }
    STAMP(4);
#define WAIT_GO() do { _Pragma("unroll 1") for (unsigned spin = 0; spin < SPIN_CAP; ++spin) { POLL_FENCE(); \
        const unsigned e_ = __builtin_amdgcn_readfirstlane(__hip_atomic_load(E + chunk, RLX_AGENT)); \
        if (e_ == nonce + 2u) break; \
        if (e_ == nonce && lane == 0) __hip_atomic_store(R + chunk, nonce + 1u, RLX_AGENT); \
        __builtin_amdgcn_s_sleep(32); } } while (0)
    if (gu || dn) {
      if (dn) {
        lds_wait(&lds[L_SC + 3], 3);
        WAIT_GO();
        v2u pv[4];
        const int go = WS_GU + (sl >> 1) * 4096 + (sl & 1) * 128, gvo = r * 256 + c * 8;
#pragma unroll 1
        for (unsigned spin = 0; spin < SPIN_CAP; ++spin) {
          POLL_FENCE();
          bool ok = true;
#pragma unroll
          for (int k = 0; k < 4; ++k) { pv[k] = __builtin_amdgcn_raw_buffer_load_b64(rw, gvo, go + k * 1024, AUX_SC1); }
#pragma unroll
          for (int k = 0; k < 4; ++k) ok = ok && pv[k][0] != SENT && pv[k][1] != SENT;
          if (__all(ok)) break;
          __builtin_amdgcn_s_sleep(32);
        }
        const float g0 = __uint_as_float(pv[0][0]) + __uint_as_float(pv[1][0]), g1 = __uint_as_float(pv[0][1]) + __uint_as_float(pv[1][1]);
        const float u0 = __uint_as_float(pv[2][0]) + __uint_as_float(pv[3][0]), u1 = __uint_as_float(pv[2][1]) + __uint_as_float(pv[3][1]);
        ((unsigned*)lds)[L_XT + (256 + c) * 4 + r] = pk_bf16(sigmoidf_(g0) * u0, sigmoidf_(g1) * u1);
        asm volatile("s_waitcnt lgkmcnt(0)" ::: "memory");
      }
      const float* xt = &lds[L_XT + (dn ? 256 * 4 : ((rho & 1) * 128 + (lane >> 3)) * 4)];
      const int xstep = dn ? 4 : 32;
      float acc[4][8];
#pragma unroll
      for (int bb = 0; bb < 4; ++bb)
#pragma unroll
        for (int k = 0; k < 8; ++k) acc[bb][k] = 0.f;
#pragma unroll 1
      for (int I0 = 0; I0 < 16; I0 += 8) {
#pragma unroll
        for (int i = 0; i < 8; ++i) {
          __builtin_amdgcn_s_setprio(PRIO_MATH);
          const v4u xp = *(const v4u*)(xt + (I0 + i) * xstep);
#pragma unroll
          for (int k = 0; k < 4; ++k) {
            const unsigned lo = __builtin_amdgcn_perm(m1[i][k], m0[i][k], 0x05040100u), hi = __builtin_amdgcn_perm(m1[i][k], m0[i][k], 0x07060302u);
#pragma unroll
            for (int bb = 0; bb < 4; ++bb) { acc[bb][2 * k] = dot2(lo, xp[bb], acc[bb][2 * k]); acc[bb][2 * k + 1] = dot2(hi, xp[bb], acc[bb][2 * k + 1]); }
          }
          KEEP8(acc[0]); KEEP8(acc[1]); KEEP8(acc[2]); KEEP8(acc[3]);
          SB();
          __builtin_amdgcn_s_setprio(3);
          const int o = I0 == 0 ? mso + (8 + i) * istep : 0x40000000;
          m0[i] = __builtin_amdgcn_raw_buffer_load_b128(rm, mvo, o, AUX_NT); SB();
          m1[i] = __builtin_amdgcn_raw_buffer_load_b128(rm, mvo, o + rowb, AUX_NT); SB();
        }
      }
      if (!dn) {
#pragma unroll
        for (int bb = 0; bb < 4; ++bb) {
          *(f32x4*)&lds[L_YP + lane * 32 + bb * 8] = f32x4{acc[bb][0], acc[bb][1], acc[bb][2], acc[bb][3]};
          *(f32x4*)&lds[L_YP + lane * 32 + bb * 8 + 4] = f32x4{acc[bb][4], acc[bb][5], acc[bb][6], acc[bb][7]};
        }
        asm volatile("s_waitcnt lgkmcnt(0)" ::: "memory");
        f32x4 g4 = {0.f, 0.f, 0.f, 0.f};
        const float* sp = &lds[L_YP + (c >> 1) * 32 + r * 8 + (c & 1) * 4];
#pragma unroll
        for (int rg = 0; rg < 8; ++rg) g4 += *(const f32x4*)(sp + rg * 256);
        __builtin_amdgcn_raw_buffer_store_b128(v4u{canon(g4[0]), canon(g4[1]), canon(g4[2]), canon(g4[3])}, rw, lane * 16, WS_GU + rho * 1024, AUX_SC1);
      } else {
#pragma unroll
        for (int bb = 0; bb < 4; ++bb) {
          const float f = 1.0f - gate[bb];
          const v4u y0 = {canon(acc[bb][0] * f), canon(acc[bb][1] * f), canon(acc[bb][2] * f), canon(acc[bb][3] * f)};
          const v4u y1 = {canon(acc[bb][4] * f), canon(acc[bb][5] * f), canon(acc[bb][6] * f), canon(acc[bb][7] * f)};
          __builtin_amdgcn_raw_buffer_store_b128(y0, rw, lane * 32, WS_YM + sl * 8192 + bb * 2048, AUX_SC1);
          __builtin_amdgcn_raw_buffer_store_b128(y1, rw, lane * 32 + 16, WS_YM + sl * 8192 + bb * 2048, AUX_SC1);
        }
      }
    }
    STAMP(1);
    lds_wait(&lds[L_SC + 2], NW);
    const float mw = lds[L_M + (lane & 7)];
    float M = fmaxf(mw, dpp<XOR1>(mw)); M = fmaxf(M, dpp<XOR2>(M)); M = fmaxf(M, dpp<XOR7>(M));
    const float ew = __builtin_amdgcn_exp2f(mw - M);
    float L = ew * lds[L_L + (lane & 7)]; L += dpp<XOR1>(L); L += dpp<XOR2>(L); L += dpp<XOR7>(L);
    if (lane == 0) __hip_atomic_store(ML + chunk, ((unsigned long long)canon(L) << 32) | canon(M), RLX_AGENT);
    if (!dn) WAIT_GO();
    lds_wait(&lds[L_SC + 3], 6);
    unsigned long long pm = ~0ull;
#pragma unroll 1
    for (unsigned spin = 0; spin < SPIN_CAP; ++spin) {
      POLL_FENCE();
      pm = __hip_atomic_load(ML + (chunk ^ 1), RLX_AGENT);
      if (__builtin_amdgcn_readfirstlane((unsigned)pm) != SENT) break;
      __builtin_amdgcn_s_sleep(32);
    }
    {
      const float M2 = __uint_as_float((unsigned)pm), L2 = __uint_as_float((unsigned)(pm >> 32));
      const float Mx = fmaxf(M, M2), e1 = __builtin_amdgcn_exp2f(M - Mx), e2 = __builtin_amdgcn_exp2f(M2 - Mx);
      if (lane < 8) lds[L_F + lane] = ew * gate_b * e1 / (e1 * L + e2 * L2);
    }
    STAMP(2);
    const int u = chunk & 63;
    float xs[8];
#pragma unroll
    for (int k = 0; k < 8; ++k) xs[k] = 0.f;
#pragma unroll 1
    for (int round = 0; round < 2; ++round) {
      const int yo = round ? WS_Y + (b * 64) * 2048 + u * 32 : WS_YM + b * 2048 + u * 32;
      const int vo = round ? lane * 2048 : lane * 8192;
      v4u y0 = {0u, 0u, 0u, 0u}, y1 = y0;
      bool ok = !round && (NOMLP || lane >= NSL);
      BAR();
      if (round) { STAMP(3); }
#pragma unroll 1
      for (unsigned spin = 0; spin < SPIN_CAP; ++spin) {
        POLL_FENCE();
        if (!ok) {
          y0 = __builtin_amdgcn_raw_buffer_load_b128(rw, vo, yo, AUX_SC1);
          y1 = __builtin_amdgcn_raw_buffer_load_b128(rw, vo, yo + 16, AUX_SC1);
          ok = y0[0] != SENT && y0[1] != SENT && y0[2] != SENT && y0[3] != SENT && y1[0] != SENT && y1[1] != SENT && y1[2] != SENT && y1[3] != SENT;
        }
        if (__all(ok)) break;
        __builtin_amdgcn_s_sleep(2);
      }
#pragma unroll
      for (int k = 0; k < 4; ++k) { xs[k] += __uint_as_float(y0[k]); xs[4 + k] += __uint_as_float(y1[k]); }
    }
#define TR_STEP(HALF, CTRL, BIT) _Pragma("unroll") for (int hh = 0; hh < (HALF); ++hh) { \
      const float lo_ = xs[hh], hi_ = xs[hh + (HALF)]; const float t1_ = lo_ + dpp<CTRL>(lo_), t2_ = hi_ + dpp<CTRL>(hi_); \
      xs[hh] = (c & (BIT)) ? t2_ : t1_; }
    TR_STEP(4, XOR8, 8) TR_STEP(2, XOR7, 4) TR_STEP(1, XOR2, 2)
    float o = xs[0]; o += dpp<XOR1>(o); o = xrow_sum(o);
    if (lane < 16 && !(lane & 1)) out[b * DM + u * 8 + (lane >> 1)] = __builtin_bit_cast(unsigned short, (__bf16)o);
    STAMP(7);
    return;
  }

  const __amdgpu_buffer_rsrc_t rk = __builtin_amdgcn_make_buffer_rsrc((void*)kc, 0, NB * HQ * SK * DH * 2, RSW3);
  const __amdgpu_buffer_rsrc_t rv = __builtin_amdgcn_make_buffer_rsrc((void*)vc, 0, NB * HQ * SK * DH * 2, RSW3);
  const __amdgpu_buffer_rsrc_t rq = __builtin_amdgcn_make_buffer_rsrc((void*)wq, 0, ZQ ? 0 : DM * HQ * DH * 2, RSW3);
  const __amdgpu_buffer_rsrc_t ro = __builtin_amdgcn_make_buffer_rsrc((void*)wo, 0, ZO ? 0 : DM * HQ * DH * 2, RSW3);

  const int kvoff = lane * 16;
  const int kvbase = chunk * (512 * 1024) + w * 4096;
  const int r0 = chunk * 4;
#define KVOFF(t) (kvbase + ((((t) + r0) & 15) << 15))
  v4u wq_[16], wt[16];
  v4u a[4 * RG];
  unsigned xd[8];
  const int oso = (h * DH + 16 * w) * (DM * 2);
  const int qvoff = r * 2 * (HQ * DH * 2) + c * 16, qso = 8 * w * (HQ * DH * 2) + h * DH * 2;
  const int rot = STAGGER ? 2 * b : m8;
#define KLOAD(n) { a[n] = __builtin_amdgcn_raw_buffer_load_b128(rk, kvoff, KVOFF((n) >> 2) + ((n) & 3) * 1024, AUX_K); SB(); }
#define LD_O(i) { wt[i] = __builtin_amdgcn_raw_buffer_load_b128(ro, kvoff, oso + (((i) + 2 * m8) & 15) * (DM * 2), AUX_W); SB(); }
#define LD_Q(i) { wq_[i] = __builtin_amdgcn_raw_buffer_load_b128(rq, qvoff, qso + (64 * ((((i) >> 1) + rot) & 7) + ((i) & 1)) * (HQ * DH * 2), AUX_WQ); SB(); }
#define LD_X() { _Pragma("unroll") for (int k = 0; k < 8; ++k) xd[k] = __builtin_amdgcn_raw_buffer_load_b32(rx, r * 4, (b * DM + 64 * ((k + rot) & 7) + 8 * w) * 2, 0); SB(); }
#define QPROJ() { \
    float qa[8]; \
    _Pragma("unroll") for (int k = 0; k < 8; ++k) qa[k] = 0.f; \
    _Pragma("unroll") for (int i = 0; i < 16; ++i) { \
      const unsigned xx = xd[i >> 1]; \
      const unsigned xl = (i & 1) ? (xx >> 16) : (xx & 0xffffu), xh = (i & 1) ? (xx & 0xffff0000u) : (xx << 16); \
      _Pragma("unroll") for (int k = 0; k < 4; ++k) { qa[2 * k] = dot2(wq_[i][k], xl, qa[2 * k]); qa[2 * k + 1] = dot2(wq_[i][k], xh, qa[2 * k + 1]); } \
    } \
    _Pragma("unroll") for (int k = 0; k < 8; ++k) qa[k] = xrow_sum(qa[k]); \
    KEEP8(qa); \
    if (r == 0) { \
      *(f32x4*)&lds[L_QP + w * 128 + c * 8] = f32x4{qa[0], qa[1], qa[2], qa[3]}; \
      *(f32x4*)&lds[L_QP + w * 128 + c * 8 + 4] = f32x4{qa[4], qa[5], qa[6], qa[7]}; \
    } \
    SB(); }
  if (!STAGGER || s == 0) {
#pragma unroll
    for (int i = 0; i < 4; ++i) LD_Q(i)
    LD_X()
#pragma unroll
    for (int i = 4; i < 16; ++i) LD_Q(i)
    BAR();
    SB();
    STAMP(1);
#pragma unroll
    for (int n = 0; n < K0; ++n) KLOAD(n)
    QPROJ()
#pragma unroll
    for (int n = K0; n < 4 * RG; ++n) KLOAD(n)
  } else {
#pragma unroll
    for (int n = 0; n < 4 * RG; ++n) KLOAD(n)
    BAR();
    SB();
    STAMP(1);
    LD_X()
#pragma unroll
    for (int i = 0; i < 16; ++i) LD_Q(i)
    QPROJ()
  }
  BAR();
  STAMP(2);
  unsigned qpk[4];
  {
    f32x4 q0 = {0.f, 0.f, 0.f, 0.f}, q1 = q0;
#pragma unroll
    for (int ww = 0; ww < NW; ++ww) { q0 += *(const f32x4*)&lds[L_QP + ww * 128 + c * 8]; q1 += *(const f32x4*)&lds[L_QP + ww * 128 + c * 8 + 4]; }
    qpk[0] = pk_bf16(q0[0], q0[1]); qpk[1] = pk_bf16(q0[2], q0[3]); qpk[2] = pk_bf16(q1[0], q1[1]); qpk[3] = pk_bf16(q1[2], q1[3]);
    asm volatile("" : "+v"(qpk[0]), "+v"(qpk[1]), "+v"(qpk[2]), "+v"(qpk[3]));
    SB();
  }
  STAMP(3);
  float* prow = &lds[L_P + (w * 4 + r) * 64];
  float mx = -INFINITY;
#define KSTEP(T, RN, AUXN) { \
    float xs[16]; \
    _Pragma("unroll") for (int g = 0; g < 4; ++g) { \
      const int o = KVOFF(4 * (T) + g + RG); \
      _Pragma("unroll") for (int i = 0; i < 4; ++i) { \
        v4u& kk = a[(g % RG) * 4 + i]; \
        float d = dot2(kk[0], qpk[0], 0.f); d = dot2(kk[1], qpk[1], d); d = dot2(kk[2], qpk[2], d); d = dot2(kk[3], qpk[3], d); \
        asm volatile("" : "+v"(d)); \
        xs[g * 4 + i] = d; \
        SB(); \
        kk = __builtin_amdgcn_raw_buffer_load_b128(RN, kvoff, o + i * 1024, AUXN); \
        SB(); \
      } \
      LD_O(4 * (T) + g) \
    } \
    TR_STEP(8, XOR8, 8) TR_STEP(4, XOR7, 4) TR_STEP(2, XOR2, 2) TR_STEP(1, XOR1, 1) \
    const float sc = xs[0] * (SCALE * LOG2E); \
    prow[16 * (T) + c] = sc; \
    mx = fmaxf(mx, sc); \
    if (tid == 64 * (NW - 1)) __hip_atomic_store((lu32*)&lds[L_SC + 3], (unsigned)(T) + 1u, __ATOMIC_RELAXED, __HIP_MEMORY_SCOPE_WORKGROUP); }
  static_assert(RG == 4, "the last K step refills the whole ring from V");
  KSTEP(0, rk, AUX_K) KSTEP(1, rk, AUX_K) KSTEP(2, rk, AUX_K) KSTEP(3, rv, AUX_V)
  STAMP(4);
  mx = xrow_max(row_max(mx));
  {
    float ls = 0.f;
#pragma unroll
    for (int T = 0; T < 4; ++T) { const float p = __builtin_amdgcn_exp2f(prow[16 * T + c] - mx); prow[16 * T + c] = p; ls += p; }
    ls = xrow_sum(row_sum(ls));
    if (lane == 0) { lds[L_M + w] = mx; lds[L_L + w] = ls; lds_arrive(&lds[L_SC + 2]); }
  }
  asm volatile("s_waitcnt lgkmcnt(0)" ::: "memory");
  SB();
  STAMP(5);
  float oa[8];
#pragma unroll
  for (int k = 0; k < 8; ++k) oa[k] = 0.f;
#pragma unroll 1
  for (int T = 0; T < 4; ++T) {
    if (T == PACE) lds_wait(&lds[L_SC + 2], NW);
#pragma unroll
    for (int g = 0; g < 4; ++g) {
      const int un = 4 * T + g + RG;
      const int o = un < 16 ? KVOFF(un) : 0x40000000;
      const f32x4 p4 = *(const f32x4*)&prow[16 * T + 4 * g];
#pragma unroll
      for (int i = 0; i < 4; ++i) {
        v4u& vv = a[(g % RG) * 4 + i];
#pragma unroll
        for (int k = 0; k < 4; ++k) { oa[2 * k] = fmaf(p4[i], bflo(vv[k]), oa[2 * k]); oa[2 * k + 1] = fmaf(p4[i], bfhi(vv[k]), oa[2 * k + 1]); }
        KEEP8(oa);
        SB();
        vv = __builtin_amdgcn_raw_buffer_load_b128(rv, kvoff, o + i * 1024, AUX_V);
        SB();
      }
    }
    if (tid == 64 * (NW - 1)) __hip_atomic_store((lu32*)&lds[L_SC + 3], (unsigned)T + 5u, __ATOMIC_RELAXED, __HIP_MEMORY_SCOPE_WORKGROUP);
  }
#pragma unroll
  for (int k = 0; k < 8; ++k) oa[k] = xrow_sum(oa[k]);
  STAMP(6);
  if (r == 0) {
    *(f32x4*)&lds[L_OP + w * 128 + c * 8] = f32x4{oa[0], oa[1], oa[2], oa[3]};
    *(f32x4*)&lds[L_OP + w * 128 + c * 8 + 4] = f32x4{oa[4], oa[5], oa[6], oa[7]};
  }
  BAR();
  float od = 0.f;
#pragma unroll
  for (int ww = 0; ww < NW; ++ww) od = fmaf(lds[L_F + ww], lds[L_OP + ww * 128 + 16 * w + ((c + 2 * m8) & 15)], od);
  float ya[8];
#pragma unroll
  for (int k = 0; k < 8; ++k) ya[k] = 0.f;
#pragma unroll
  for (int i = 0; i < 16; ++i) {
    const float ov = __builtin_bit_cast(float, __builtin_amdgcn_readlane(__builtin_bit_cast(int, od), i));
#pragma unroll
    for (int k = 0; k < 4; ++k) { ya[2 * k] = fmaf(ov, bflo(wt[i][k]), ya[2 * k]); ya[2 * k + 1] = fmaf(ov, bfhi(wt[i][k]), ya[2 * k + 1]); }
  }
  *(f32x4*)&lds[L_YP + w * 512 + lane * 8] = f32x4{ya[0], ya[1], ya[2], ya[3]};
  *(f32x4*)&lds[L_YP + w * 512 + lane * 8 + 4] = f32x4{ya[4], ya[5], ya[6], ya[7]};
  BAR();
  if (w < 2) {
    f32x4 y = *(const f32x4*)&lds[L_YP + tid * 4];
#pragma unroll
    for (int ww = 1; ww < NW; ++ww) y += *(const f32x4*)&lds[L_YP + ww * 512 + tid * 4];
    const v4u yu = {canon(y[0]), canon(y[1]), canon(y[2]), canon(y[3])};
    __builtin_amdgcn_raw_buffer_store_b128(yu, rw, tid * 16, WS_Y + chunk * 2048, AUX_SC1);
  }
}
}

extern "C" void kernel_launch(void* const* d_in, const int* in_sizes, int n_in, void* d_out, int out_size, void* d_ws, size_t ws_size,
                              hipStream_t stream) {
  typedef const unsigned short* P;
  hipLaunchKernelGGL(k_main, dim3(NWG), dim3(NTHR), 0, stream, (P)d_in[0], (P)d_in[1], (P)d_in[2], (P)d_in[3], (P)d_in[4], (P)d_in[6], (P)d_in[7],
                     (P)d_in[8], (P)d_in[9], (unsigned short*)d_out, (unsigned char*)d_ws);
}
```

```cpp
#include <hip/hip_runtime.h>
#include <hip/hip_bf16.h>
#include <cstdint>

namespace {
typedef unsigned v4u __attribute__((ext_vector_type(4)));
typedef unsigned v2u __attribute__((ext_vector_type(2)));
typedef float f32x4 __attribute__((ext_vector_type(4)));
typedef __bf16 bf16x2 __attribute__((ext_vector_type(2)));
#define GAS __attribute__((address_space(1)))
typedef GAS unsigned gu32;
typedef GAS unsigned long long gu64;
#define RLX_AGENT __ATOMIC_RELAXED, __HIP_MEMORY_SCOPE_AGENT

constexpr int NB = 4, DM = 512, HQ = 32, SK = 4096, DH = 128, FF = 1376;
constexpr int NWG = 256, NW = 8, NTHR = 64 * (NW + 1);
constexpr int NSL = 43;
constexpr int RSW3 = 0x00020000;
constexpr int AUX_NT = 2, AUX_SC1 = 16;
constexpr float LOG2E = 1.4426950408889634f;
constexpr float SCALE = 0.08838834764831845f;
constexpr unsigned SENT = 0xFFFFFFFFu;
constexpr unsigned SPIN_CAP = 1u << 18;

#define AUX_WQ 0
#define AUX_W 0
#define RG 4
#define AUX_K 2
#define AUX_V 2
#define PRIO_MATH 0
#define PACE 0
#define STAGGER 1
#define NOMLP 0
#define ZQ 0
#define ZO 0
#define SLACK 1
#define ROTM 0
#define MAPM 0
#define MCONT 0
#define ZM 0
#define AUX_M 2
#define K0 4

constexpr int WS_R = 0;
constexpr int WS_E = 4096;
constexpr int WS_CONF = 8192;
constexpr int WS_ML = 12288;
constexpr int WS_Y = 16384;
constexpr int WS_YM = WS_Y + 256 * 2048;
constexpr int WS_GU = WS_YM + NSL * 8192;
constexpr int WS_END = WS_GU + 22 * 4096;

__device__ __forceinline__ float dot2(unsigned a, unsigned b, float acc) {
  return __builtin_amdgcn_fdot2_f32_bf16(__builtin_bit_cast(bf16x2, a), __builtin_bit_cast(bf16x2, b), acc, false);
}
__device__ __forceinline__ float bflo(unsigned u) { return __builtin_bit_cast(float, u << 16); }
__device__ __forceinline__ float bfhi(unsigned u) { return __builtin_bit_cast(float, u & 0xffff0000u); }
__device__ __forceinline__ unsigned pk_bf16(float lo, float hi) {
  bf16x2 r; r.x = (__bf16)lo; r.y = (__bf16)hi; return __builtin_bit_cast(unsigned, r);
}
template <int CTRL> __device__ __forceinline__ float dpp(float x) {
  return __builtin_bit_cast(float, __builtin_amdgcn_mov_dpp(__builtin_bit_cast(int, x), CTRL, 0xf, 0xf, true));
}
constexpr int XOR1 = 0xB1, XOR2 = 0x4E, XOR7 = 0x141, XOR8 = 0x128, ROR4 = 0x124;
__device__ __forceinline__ float xrow_sum(float x) {
  auto s = __builtin_amdgcn_permlane16_swap(__float_as_uint(x), __float_as_uint(x), false, false);
  x = __uint_as_float(s[0]) + __uint_as_float(s[1]);
  auto t = __builtin_amdgcn_permlane32_swap(__float_as_uint(x), __float_as_uint(x), false, false);
  return __uint_as_float(t[0]) + __uint_as_float(t[1]);
}
__device__ __forceinline__ float xrow_max(float x) {
  auto s = __builtin_amdgcn_permlane16_swap(__float_as_uint(x), __float_as_uint(x), false, false);
  x = fmaxf(__uint_as_float(s[0]), __uint_as_float(s[1]));
  auto t = __builtin_amdgcn_permlane32_swap(__float_as_uint(x), __float_as_uint(x), false, false);
  return fmaxf(__uint_as_float(t[0]), __uint_as_float(t[1]));
}
__device__ __forceinline__ float row_sum(float x) { x += dpp<XOR1>(x); x += dpp<XOR2>(x); x += dpp<XOR7>(x); x += dpp<XOR8>(x); return x; }
__device__ __forceinline__ float row_max(float x) { x = fmaxf(x, dpp<XOR1>(x)); x = fmaxf(x, dpp<XOR2>(x)); x = fmaxf(x, dpp<XOR7>(x)); x = fmaxf(x, dpp<XOR8>(x)); return x; }
__device__ __forceinline__ float sigmoidf_(float x) { return 1.0f / (1.0f + __expf(-x)); }
__device__ __forceinline__ unsigned canon(float v) { const unsigned u = __float_as_uint(v); return u == SENT ? 0x7FC00000u : u; }
#define SB() __builtin_amdgcn_sched_barrier(0)
#define POLL_FENCE() asm volatile("" ::: "memory")
#define DRAIN() asm volatile("s_waitcnt vmcnt(0)" ::: "memory")
#define BAR() do { asm volatile("s_waitcnt lgkmcnt(0)" ::: "memory"); __builtin_amdgcn_s_barrier(); asm volatile("" ::: "memory"); } while (0)
#define KEEP8(a) asm volatile("" : "+v"(a[0]), "+v"(a[1]), "+v"(a[2]), "+v"(a[3]), "+v"(a[4]), "+v"(a[5]), "+v"(a[6]), "+v"(a[7]))
#define STAMP(k) do { } while (0)

constexpr int L_QP = 0;
constexpr int L_P = L_QP + 8 * 128;
constexpr int L_M = L_P + 8 * 256;
constexpr int L_L = L_M + 8;
constexpr int L_SC = L_L + 8;
constexpr int L_F = L_SC + 8;
constexpr int L_OP = L_F + 8;
constexpr int L_YP = L_OP + 8 * 128;
constexpr int L_XT = L_YP + 8 * 512;
constexpr int L_END = L_XT + 272 * 4;
typedef __attribute__((address_space(3))) unsigned lu32;
__device__ __forceinline__ void lds_arrive(float* p) { asm volatile("s_waitcnt lgkmcnt(0)" ::: "memory"); __hip_atomic_fetch_add((lu32*)p, 1u, __ATOMIC_RELAXED, __HIP_MEMORY_SCOPE_WORKGROUP); }
__device__ __forceinline__ void lds_wait(float* p, unsigned n) {
#pragma unroll 1
  for (unsigned spin = 0; spin < (SPIN_CAP << 4); ++spin) {
    if (__builtin_amdgcn_readfirstlane(__hip_atomic_load((lu32*)p, __ATOMIC_RELAXED, __HIP_MEMORY_SCOPE_WORKGROUP)) >= n) break;
    __builtin_amdgcn_s_sleep(1);
  }
  asm volatile("" ::: "memory");
}

__global__ void __launch_bounds__(NTHR, 3)
k_main(const unsigned short* __restrict__ x, const unsigned short* __restrict__ kc, const unsigned short* __restrict__ vc,
       const unsigned short* __restrict__ wq, const unsigned short* __restrict__ wo, const unsigned short* __restrict__ wg,
       const unsigned short* __restrict__ wu, const unsigned short* __restrict__ wd, const unsigned short* __restrict__ gw,
       unsigned short* __restrict__ out, unsigned char* __restrict__ ws) {
  __shared__ __attribute__((aligned(16))) float lds[L_END];
  const int tid = threadIdx.x, lane = tid & 63, w = __builtin_amdgcn_readfirstlane(tid >> 6), r = lane >> 4, c = lane & 15;
  const int bid = blockIdx.x, xg = bid & 7, j = bid >> 3;
  const int h = xg * 4 + (j >> 3), b = (j >> 1) & 3, s = j & 1, m8 = j & 7;
  const int chunk = (b * HQ + h) * 2 + s;
  const __amdgpu_buffer_rsrc_t rx = __builtin_amdgcn_make_buffer_rsrc((void*)x, 0, NB * DM * 2, RSW3);
  const __amdgpu_buffer_rsrc_t rw = __builtin_amdgcn_make_buffer_rsrc((void*)ws, 0, WS_END, RSW3);
  STAMP(0);

  if (w == NW) {
    gu32* R = (gu32*)(ws + WS_R); gu32* E = (gu32*)(ws + WS_E); gu64* ML = (gu64*)(ws + WS_ML);
    __builtin_amdgcn_s_setprio(3);
    if (lane < 3) ((unsigned*)lds)[L_SC + 1 + lane] = 0u;
    const int rho = NOMLP ? 999 : (bid & 1) ? (bid < 8 ? 127 + (bid >> 1) : 999) : (bid >> 1) - 1;
    const bool gu = (unsigned)rho < 88u, dn = rho >= 88 && rho < 88 + NSL;
    const int sl = rho - 88;
    const unsigned r_old = __hip_atomic_load(R + chunk, RLX_AGENT), e_old = __hip_atomic_load(E + chunk, RLX_AGENT);
    v4u rfirst = __builtin_amdgcn_raw_buffer_load_b128(rw, lane * 16, bid == 0 ? WS_R : 0x40000000, AUX_SC1);
    v4u xv[4];
#pragma unroll
    for (int bb = 0; bb < 4; ++bb) xv[bb] = __builtin_amdgcn_raw_buffer_load_b128(rx, lane * 16, bb * DM * 2, 0);
    const v4u gv = *(const GAS v4u*)((const GAS unsigned char*)gw + lane * 16);
    BAR();
    BAR();
    const v4u sent4 = {SENT, SENT, SENT, SENT};
    __builtin_amdgcn_raw_buffer_store_b128(sent4, rw, lane * 16, WS_Y + chunk * 2048, AUX_SC1);
    __builtin_amdgcn_raw_buffer_store_b128(sent4, rw, lane * 16, WS_Y + chunk * 2048 + 1024, AUX_SC1);
    if (dn) {
#pragma unroll 1
      for (int i = 0; i < 8; ++i) __builtin_amdgcn_raw_buffer_store_b128(sent4, rw, lane * 16, WS_YM + sl * 8192 + i * 1024, AUX_SC1);
    }
    if (gu) __builtin_amdgcn_raw_buffer_store_b128(sent4, rw, lane * 16, WS_GU + rho * 1024, AUX_SC1);
    if (lane == 0) __hip_atomic_store(ML + chunk, ~0ull, RLX_AGENT);
    unsigned nonce = (r_old & ~3u) + 4u; if (e_old - nonce < 4u) nonce += 4u;
    float gate[4];
#pragma unroll
    for (int bb = 0; bb < 4; ++bb) {
      float gd = dot2(xv[bb][0], gv[0], 0.f); gd = dot2(xv[bb][1], gv[1], gd); gd = dot2(xv[bb][2], gv[2], gd); gd = dot2(xv[bb][3], gv[3], gd);
      gate[bb] = sigmoidf_(xrow_sum(row_sum(gd)));
    }
    const float gate_b = b == 0 ? gate[0] : b == 1 ? gate[1] : b == 2 ? gate[2] : gate[3];
#pragma unroll
    for (int k = 0; k < 4; ++k) *(v4u*)&lds[L_XT + (4 * lane + k) * 4] = v4u{xv[0][k], xv[1][k], xv[2][k], xv[3][k]};
    DRAIN();
    if (lane == 0) __hip_atomic_store(R + chunk, nonce, RLX_AGENT);
    const __amdgpu_buffer_rsrc_t rm = __builtin_amdgcn_make_buffer_rsrc((void*)(dn ? wd : (rho & 2) ? wu : wg), 0, ((gu || dn) && !ZM) ? DM * FF * 2 : 0, RSW3);
    const int rowb = (dn || MCONT) ? DM * 2 : FF * 2;
    const int mvo = (dn || MCONT) ? lane * 16 : (lane >> 3) * 2 * (FF * 2) + (lane & 7) * 16;
    const int mso = dn ? sl * 32 * (DM * 2) : MCONT ? ((rho >> 2) * 2 + (rho & 1)) * 32000 : (rho & 1) * 256 * (FF * 2) + (rho >> 2) * 128, istep = (dn || MCONT) ? 2 * (DM * 2) : 16 * (FF * 2);
    v4u m0[8], m1[8];
    if (gu || dn) {
#pragma unroll
      for (int i = 0; i < 8; ++i) {
        m0[i] = __builtin_amdgcn_raw_buffer_load_b128(rm, mvo, mso + i * istep, AUX_M); SB();
        m1[i] = __builtin_amdgcn_raw_buffer_load_b128(rm, mvo, mso + i * istep + rowb, AUX_M); SB();
      }
    }
    if (bid == 0) {
      unsigned seen = 0;
#pragma unroll 1
      for (unsigned spin = 0; spin < SPIN_CAP; ++spin) {
        POLL_FENCE();
        __builtin_amdgcn_s_sleep(40);
        v4u rq = __builtin_amdgcn_raw_buffer_load_b128(rw, lane * 16, WS_R, AUX_SC1);
#pragma unroll
        for (int k = 0; k < 4; ++k) seen |= (rq[k] != rfirst[k]) ? 1u << k : 0u;
        if (__all(seen == 15u)) {
          __builtin_amdgcn_raw_buffer_store_b128((rq & ~3u) + 2u, rw, lane * 16, WS_E, AUX_SC1);
          break;
        }
        if (spin >= 2) {
#pragma unroll
          for (int k = 0; k < 4; ++k) if (!(seen >> k & 1u)) __hip_atomic_store(E + lane * 4 + k, rq[k], RLX_AGENT);
        }
      }
    }
    STAMP(4);
#define WAIT_GO() do { _Pragma("unroll 1") for (unsigned spin = 0; spin < SPIN_CAP; ++spin) { POLL_FENCE(); \
        const unsigned e_ = __builtin_amdgcn_readfirstlane(__hip_atomic_load(E + chunk, RLX_AGENT)); \
        if (e_ == nonce + 2u) break; \
        if (e_ == nonce && lane == 0) __hip_atomic_store(R + chunk, nonce + 1u, RLX_AGENT); \
        __builtin_amdgcn_s_sleep(32); } } while (0)
    if (gu || dn) {
      if (dn) {
        lds_wait(&lds[L_SC + 3], 3);
        WAIT_GO();
        v2u pv[4];
        const int go = WS_GU + (sl >> 1) * 4096 + (sl & 1) * 128, gvo = r * 256 + c * 8;
#pragma unroll 1
        for (unsigned spin = 0; spin < SPIN_CAP; ++spin) {
          POLL_FENCE();
          bool ok = true;
#pragma unroll
          for (int k = 0; k < 4; ++k) { pv[k] = __builtin_amdgcn_raw_buffer_load_b64(rw, gvo, go + k * 1024, AUX_SC1); }
#pragma unroll
          for (int k = 0; k < 4; ++k) ok = ok && pv[k][0] != SENT && pv[k][1] != SENT;
          if (__all(ok)) break;
          __builtin_amdgcn_s_sleep(32);
        }
        const float g0 = __uint_as_float(pv[0][0]) + __uint_as_float(pv[1][0]), g1 = __uint_as_float(pv[0][1]) + __uint_as_float(pv[1][1]);
        const float u0 = __uint_as_float(pv[2][0]) + __uint_as_float(pv[3][0]), u1 = __uint_as_float(pv[2][1]) + __uint_as_float(pv[3][1]);
        ((unsigned*)lds)[L_XT + (256 + c) * 4 + r] = pk_bf16(sigmoidf_(g0) * u0, sigmoidf_(g1) * u1);
        asm volatile("s_waitcnt lgkmcnt(0)" ::: "memory");
      }
      const float* xt = &lds[L_XT + (dn ? 256 * 4 : ((rho & 1) * 128 + (lane >> 3)) * 4)];
      const int xstep = dn ? 4 : 32;
      float acc[4][8];
#pragma unroll
      for (int bb = 0; bb < 4; ++bb)
#pragma unroll
        for (int k = 0; k < 8; ++k) acc[bb][k] = 0.f;
#pragma unroll 1
      for (int I0 = 0; I0 < 16; I0 += 8) {
#pragma unroll
        for (int i = 0; i < 8; ++i) {
          __builtin_amdgcn_s_setprio(PRIO_MATH);
          const v4u xp = *(const v4u*)(xt + (I0 + i) * xstep);
#pragma unroll
          for (int k = 0; k < 4; ++k) {
            const unsigned lo = __builtin_amdgcn_perm(m1[i][k], m0[i][k], 0x05040100u), hi = __builtin_amdgcn_perm(m1[i][k], m0[i][k], 0x07060302u);
#pragma unroll
            for (int bb = 0; bb < 4; ++bb) { acc[bb][2 * k] = dot2(lo, xp[bb], acc[bb][2 * k]); acc[bb][2 * k + 1] = dot2(hi, xp[bb], acc[bb][2 * k + 1]); }
          }
          KEEP8(acc[0]); KEEP8(acc[1]); KEEP8(acc[2]); KEEP8(acc[3]);
          SB();
          __builtin_amdgcn_s_setprio(3);
          const int o = I0 == 0 ? mso + (8 + i) * istep : 0x40000000;
          m0[i] = __builtin_amdgcn_raw_buffer_load_b128(rm, mvo, o, AUX_M); SB();
          m1[i] = __builtin_amdgcn_raw_buffer_load_b128(rm, mvo, o + rowb, AUX_M); SB();
        }
      }
      if (!dn) {
#pragma unroll
        for (int bb = 0; bb < 4; ++bb) {
          *(f32x4*)&lds[L_YP + lane * 32 + bb * 8] = f32x4{acc[bb][0], acc[bb][1], acc[bb][2], acc[bb][3]};
          *(f32x4*)&lds[L_YP + lane * 32 + bb * 8 + 4] = f32x4{acc[bb][4], acc[bb][5], acc[bb][6], acc[bb][7]};
        }
        asm volatile("s_waitcnt lgkmcnt(0)" ::: "memory");
        f32x4 g4 = {0.f, 0.f, 0.f, 0.f};
        const float* sp = &lds[L_YP + (c >> 1) * 32 + r * 8 + (c & 1) * 4];
#pragma unroll
        for (int rg = 0; rg < 8; ++rg) g4 += *(const f32x4*)(sp + rg * 256);
        __builtin_amdgcn_raw_buffer_store_b128(v4u{canon(g4[0]), canon(g4[1]), canon(g4[2]), canon(g4[3])}, rw, lane * 16, WS_GU + rho * 1024, AUX_SC1);
      } else {
#pragma unroll
        for (int bb = 0; bb < 4; ++bb) {
          const float f = 1.0f - gate[bb];
          const v4u y0 = {canon(acc[bb][0] * f), canon(acc[bb][1] * f), canon(acc[bb][2] * f), canon(acc[bb][3] * f)};
          const v4u y1 = {canon(acc[bb][4] * f), canon(acc[bb][5] * f), canon(acc[bb][6] * f), canon(acc[bb][7] * f)};
          __builtin_amdgcn_raw_buffer_store_b128(y0, rw, lane * 32, WS_YM + sl * 8192 + bb * 2048, AUX_SC1);
          __builtin_amdgcn_raw_buffer_store_b128(y1, rw, lane * 32 + 16, WS_YM + sl * 8192 + bb * 2048, AUX_SC1);
        }
      }
    }
    STAMP(1);
    lds_wait(&lds[L_SC + 2], NW);
    const float mw = lds[L_M + (lane & 7)];
    float M = fmaxf(mw, dpp<XOR1>(mw)); M = fmaxf(M, dpp<XOR2>(M)); M = fmaxf(M, dpp<XOR7>(M));
    const float ew = __builtin_amdgcn_exp2f(mw - M);
    float L = ew * lds[L_L + (lane & 7)]; L += dpp<XOR1>(L); L += dpp<XOR2>(L); L += dpp<XOR7>(L);
    if (lane == 0) __hip_atomic_store(ML + chunk, ((unsigned long long)canon(L) << 32) | canon(M), RLX_AGENT);
    if (!dn) WAIT_GO();
    lds_wait(&lds[L_SC + 3], 6);
    unsigned long long pm = ~0ull;
#pragma unroll 1
    for (unsigned spin = 0; spin < SPIN_CAP; ++spin) {
      POLL_FENCE();
      pm = __hip_atomic_load(ML + (chunk ^ 1), RLX_AGENT);
      if (__builtin_amdgcn_readfirstlane((unsigned)pm) != SENT) break;
      __builtin_amdgcn_s_sleep(32);
    }
    {
      const float M2 = __uint_as_float((unsigned)pm), L2 = __uint_as_float((unsigned)(pm >> 32));
      const float Mx = fmaxf(M, M2), e1 = __builtin_amdgcn_exp2f(M - Mx), e2 = __builtin_amdgcn_exp2f(M2 - Mx);
      if (lane < 8) lds[L_F + lane] = ew * gate_b * e1 / (e1 * L + e2 * L2);
    }
    STAMP(2);
    const int u = chunk & 63;
    float xs[8];
#pragma unroll
    for (int k = 0; k < 8; ++k) xs[k] = 0.f;
#pragma unroll 1
    for (int round = 0; round < 2; ++round) {
      const int yo = round ? WS_Y + (b * 64) * 2048 + u * 32 : WS_YM + b * 2048 + u * 32;
      const int vo = round ? lane * 2048 : lane * 8192;
      v4u y0 = {0u, 0u, 0u, 0u}, y1 = y0;
      bool ok = !round && (NOMLP || lane >= NSL);
      BAR();
      if (round) { STAMP(3); }
#pragma unroll 1
      for (unsigned spin = 0; spin < SPIN_CAP; ++spin) {
        POLL_FENCE();
        if (!ok) {
          y0 = __builtin_amdgcn_raw_buffer_load_b128(rw, vo, yo, AUX_SC1);
          y1 = __builtin_amdgcn_raw_buffer_load_b128(rw, vo, yo + 16, AUX_SC1);
          ok = y0[0] != SENT && y0[1] != SENT && y0[2] != SENT && y0[3] != SENT && y1[0] != SENT && y1[1] != SENT && y1[2] != SENT && y1[3] != SENT;
        }
        if (__all(ok)) break;
        __builtin_amdgcn_s_sleep(2);
      }
#pragma unroll
      for (int k = 0; k < 4; ++k) { xs[k] += __uint_as_float(y0[k]); xs[4 + k] += __uint_as_float(y1[k]); }
    }
#define TR_STEP(HALF, CTRL, BIT) _Pragma("unroll") for (int hh = 0; hh < (HALF); ++hh) { \
      const float lo_ = xs[hh], hi_ = xs[hh + (HALF)]; const float t1_ = lo_ + dpp<CTRL>(lo_), t2_ = hi_ + dpp<CTRL>(hi_); \
      xs[hh] = (c & (BIT)) ? t2_ : t1_; }
    TR_STEP(4, XOR8, 8) TR_STEP(2, XOR7, 4) TR_STEP(1, XOR2, 2)
    float o = xs[0]; o += dpp<XOR1>(o); o = xrow_sum(o);
    if (lane < 16 && !(lane & 1)) out[b * DM + u * 8 + (lane >> 1)] = __builtin_bit_cast(unsigned short, (__bf16)o);
    STAMP(7);
    return;
  }

  const __amdgpu_buffer_rsrc_t rk = __builtin_amdgcn_make_buffer_rsrc((void*)kc, 0, NB * HQ * SK * DH * 2, RSW3);
  const __amdgpu_buffer_rsrc_t rv = __builtin_amdgcn_make_buffer_rsrc((void*)vc, 0, NB * HQ * SK * DH * 2, RSW3);
  const __amdgpu_buffer_rsrc_t rq = __builtin_amdgcn_make_buffer_rsrc((void*)wq, 0, ZQ ? 0 : DM * HQ * DH * 2, RSW3);
  const __amdgpu_buffer_rsrc_t ro = __builtin_amdgcn_make_buffer_rsrc((void*)wo, 0, ZO ? 0 : DM * HQ * DH * 2, RSW3);

  const int kvoff = lane * 16;
  const int kvbase = chunk * (512 * 1024) + w * 4096;
  const int r0 = ROTM == 0 ? chunk * 4 : ROTM == 14 ? chunk * 4 + 4 * (chunk >> 3) : ROTM == 12 ? chunk * 4 + 4 * (chunk >> 4) : chunk * 4 + 4 * (chunk >> 6);
#define KVOFF(t) (kvbase + ((((t) + r0) & 15) << 15))
  v4u wq_[16], wt[16];
  v4u a[4 * RG];
  unsigned xd[8];
  const int oso = (h * DH + 16 * w) * (DM * 2);
  const int qvoff = r * 2 * (HQ * DH * 2) + c * 16, qso = 8 * w * (HQ * DH * 2) + h * DH * 2;
  const int rot = STAGGER ? 2 * b : m8;
#define KLOAD(n) { a[n] = __builtin_amdgcn_raw_buffer_load_b128(rk, kvoff, KVOFF((n) >> 2) + ((n) & 3) * 1024, AUX_K); SB(); }
#define LD_O(i) { wt[i] = __builtin_amdgcn_raw_buffer_load_b128(ro, kvoff, oso + (((i) + 2 * m8) & 15) * (DM * 2), AUX_W); SB(); }
#define LD_Q(i) { wq_[i] = __builtin_amdgcn_raw_buffer_load_b128(rq, qvoff, qso + (64 * ((((i) >> 1) + rot) & 7) + ((i) & 1)) * (HQ * DH * 2), AUX_WQ); SB(); }
#define LD_X() { _Pragma("unroll") for (int k = 0; k < 8; ++k) xd[k] = __builtin_amdgcn_raw_buffer_load_b32(rx, r * 4, (b * DM + 64 * ((k + rot) & 7) + 8 * w) * 2, 0); SB(); }
#define QPROJ() { \
    float qa[8]; \
    _Pragma("unroll") for (int k = 0; k < 8; ++k) qa[k] = 0.f; \
    _Pragma("unroll") for (int i = 0; i < 16; ++i) { \
      const unsigned xx = xd[i >> 1]; \
      const unsigned xl = (i & 1) ? (xx >> 16) : (xx & 0xffffu), xh = (i & 1) ? (xx & 0xffff0000u) : (xx << 16); \
      _Pragma("unroll") for (int k = 0; k < 4; ++k) { qa[2 * k] = dot2(wq_[i][k], xl, qa[2 * k]); qa[2 * k + 1] = dot2(wq_[i][k], xh, qa[2 * k + 1]); } \
    } \
    _Pragma("unroll") for (int k = 0; k < 8; ++k) qa[k] = xrow_sum(qa[k]); \
    KEEP8(qa); \
    if (r == 0) { \
      *(f32x4*)&lds[L_QP + w * 128 + c * 8] = f32x4{qa[0], qa[1], qa[2], qa[3]}; \
      *(f32x4*)&lds[L_QP + w * 128 + c * 8 + 4] = f32x4{qa[4], qa[5], qa[6], qa[7]}; \
    } \
    SB(); }
  if (!STAGGER || s == 0) {
#pragma unroll
    for (int i = 0; i < 4; ++i) LD_Q(i)
    LD_X()
#pragma unroll
    for (int i = 4; i < 16; ++i) LD_Q(i)
    BAR();
    SB();
    STAMP(1);
#pragma unroll
    for (int n = 0; n < K0; ++n) KLOAD(n)
    QPROJ()
#pragma unroll
    for (int n = K0; n < 4 * RG; ++n) KLOAD(n)
  } else {
#pragma unroll
    for (int n = 0; n < 4 * RG; ++n) KLOAD(n)
    BAR();
    SB();
    STAMP(1);
    LD_X()
#pragma unroll
    for (int i = 0; i < 16; ++i) LD_Q(i)
    QPROJ()
  }
  BAR();
  STAMP(2);
  unsigned qpk[4];
  {
    f32x4 q0 = {0.f, 0.f, 0.f, 0.f}, q1 = q0;
#pragma unroll
    for (int ww = 0; ww < NW; ++ww) { q0 += *(const f32x4*)&lds[L_QP + ww * 128 + c * 8]; q1 += *(const f32x4*)&lds[L_QP + ww * 128 + c * 8 + 4]; }
    qpk[0] = pk_bf16(q0[0], q0[1]); qpk[1] = pk_bf16(q0[2], q0[3]); qpk[2] = pk_bf16(q1[0], q1[1]); qpk[3] = pk_bf16(q1[2], q1[3]);
    asm volatile("" : "+v"(qpk[0]), "+v"(qpk[1]), "+v"(qpk[2]), "+v"(qpk[3]));
    SB();
  }
  STAMP(3);
  float* prow = &lds[L_P + (w * 4 + r) * 64];
  float mx = -INFINITY;
#define PACE_IN(st) { if (SLACK && (st) >= SLACK) lds_wait(&lds[L_SC + 1], NW * ((st) - SLACK + 1)); }
#define PACE_OUT() { if (SLACK && lane == 0) __hip_atomic_fetch_add((lu32*)&lds[L_SC + 1], 1u, __ATOMIC_RELAXED, __HIP_MEMORY_SCOPE_WORKGROUP); }
#define KSTEP(T, RN, AUXN) { \
    PACE_IN(T) \
    float xs[16]; \
    _Pragma("unroll") for (int g = 0; g < 4; ++g) { \
      const int o = KVOFF(4 * (T) + g + RG); \
      _Pragma("unroll") for (int i = 0; i < 4; ++i) { \
        v4u& kk = a[(g % RG) * 4 + i]; \
        float d = dot2(kk[0], qpk[0], 0.f); d = dot2(kk[1], qpk[1], d); d = dot2(kk[2], qpk[2], d); d = dot2(kk[3], qpk[3], d); \
        asm volatile("" : "+v"(d)); \
        xs[g * 4 + i] = d; \
        SB(); \
        kk = __builtin_amdgcn_raw_buffer_load_b128(RN, kvoff, o + i * 1024, AUXN); \
        SB(); \
      } \
      LD_O(4 * (T) + g) \
    } \
    PACE_OUT() \
    TR_STEP(8, XOR8, 8) TR_STEP(4, XOR7, 4) TR_STEP(2, XOR2, 2) TR_STEP(1, XOR1, 1) \
    const float sc = xs[0] * (SCALE * LOG2E); \
    prow[16 * (T) + c] = sc; \
    mx = fmaxf(mx, sc); \
    if (tid == 64 * (NW - 1)) __hip_atomic_store((lu32*)&lds[L_SC + 3], (unsigned)(T) + 1u, __ATOMIC_RELAXED, __HIP_MEMORY_SCOPE_WORKGROUP); }
  static_assert(RG == 4, "the last K step refills the whole ring from V");
  KSTEP(0, rk, AUX_K) KSTEP(1, rk, AUX_K) KSTEP(2, rk, AUX_K) KSTEP(3, rv, AUX_V)
  STAMP(4);
  mx = xrow_max(row_max(mx));
  {
    float ls = 0.f;
#pragma unroll
    for (int T = 0; T < 4; ++T) { const float p = __builtin_amdgcn_exp2f(prow[16 * T + c] - mx); prow[16 * T + c] = p; ls += p; }
    ls = xrow_sum(row_sum(ls));
    if (lane == 0) { lds[L_M + w] = mx; lds[L_L + w] = ls; lds_arrive(&lds[L_SC + 2]); }
  }
  asm volatile("s_waitcnt lgkmcnt(0)" ::: "memory");
  SB();
  STAMP(5);
#define WPERM(p) { _Pragma("unroll") for (int k = 0; k < 4; ++k) { const unsigned lo_ = __builtin_amdgcn_perm(wt[2 * (p) + 1][k], wt[2 * (p)][k], 0x05040100u), \
      hi_ = __builtin_amdgcn_perm(wt[2 * (p) + 1][k], wt[2 * (p)][k], 0x07060302u); wt[2 * (p)][k] = lo_; wt[2 * (p) + 1][k] = hi_; } \
      asm volatile("" : "+v"(wt[2 * (p)]), "+v"(wt[2 * (p) + 1])); }
  WPERM(0) WPERM(1) WPERM(2) WPERM(3) WPERM(4) WPERM(5)
  SB();
  float oa[8];
#pragma unroll
  for (int k = 0; k < 8; ++k) oa[k] = 0.f;
#pragma unroll 1
  for (int T = 0; T < 4; ++T) {
    if (!SLACK && T == PACE) lds_wait(&lds[L_SC + 2], NW);
    PACE_IN(4 + T)
#pragma unroll
    for (int g = 0; g < 4; ++g) {
      const int un = 4 * T + g + RG;
      const int o = un < 16 ? KVOFF(un) : 0x40000000;
      const f32x4 p4 = *(const f32x4*)&prow[16 * T + 4 * g];
#pragma unroll
      for (int i = 0; i < 4; ++i) {
        v4u& vv = a[(g % RG) * 4 + i];
#pragma unroll
        for (int k = 0; k < 4; ++k) { oa[2 * k] = fmaf(p4[i], bflo(vv[k]), oa[2 * k]); oa[2 * k + 1] = fmaf(p4[i], bfhi(vv[k]), oa[2 * k + 1]); }
        KEEP8(oa);
        SB();
        vv = __builtin_amdgcn_raw_buffer_load_b128(rv, kvoff, o + i * 1024, AUX_V);
        SB();
      }
    }
    PACE_OUT()
    if (tid == 64 * (NW - 1)) __hip_atomic_store((lu32*)&lds[L_SC + 3], (unsigned)T + 5u, __ATOMIC_RELAXED, __HIP_MEMORY_SCOPE_WORKGROUP);
  }
  WPERM(6) WPERM(7)
#pragma unroll
  for (int k = 0; k < 8; ++k) oa[k] = xrow_sum(oa[k]);
  STAMP(6);
  if (r == 0) {
    *(f32x4*)&lds[L_OP + w * 128 + c * 8] = f32x4{oa[0], oa[1], oa[2], oa[3]};
    *(f32x4*)&lds[L_OP + w * 128 + c * 8 + 4] = f32x4{oa[4], oa[5], oa[6], oa[7]};
  }
  BAR();
  float od = 0.f;
#pragma unroll
  for (int ww = 0; ww < NW; ++ww) od = fmaf(lds[L_F + ww], lds[L_OP + ww * 128 + 16 * w + ((c + 2 * m8) & 15)], od);
  const unsigned opk = pk_bf16(od, dpp<XOR1>(od));
  float ya[8];
#pragma unroll
  for (int k = 0; k < 8; ++k) ya[k] = 0.f;
#pragma unroll
  for (int p = 0; p < 8; ++p) {
    const unsigned ov = __builtin_amdgcn_readlane(opk, 2 * p);
#pragma unroll
    for (int k = 0; k < 4; ++k) { ya[2 * k] = dot2(wt[2 * p][k], ov, ya[2 * k]); ya[2 * k + 1] = dot2(wt[2 * p + 1][k], ov, ya[2 * k + 1]); }
  }
  *(f32x4*)&lds[L_YP + w * 512 + lane * 8] = f32x4{ya[0], ya[1], ya[2], ya[3]};
  *(f32x4*)&lds[L_YP + w * 512 + lane * 8 + 4] = f32x4{ya[4], ya[5], ya[6], ya[7]};
  BAR();
  if (w < 2) {
    f32x4 y = *(const f32x4*)&lds[L_YP + tid * 4];
#pragma unroll
    for (int ww = 1; ww < NW; ++ww) y += *(const f32x4*)&lds[L_YP + ww * 512 + tid * 4];
    const v4u yu = {canon(y[0]), canon(y[1]), canon(y[2]), canon(y[3])};
    __builtin_amdgcn_raw_buffer_store_b128(yu, rw, tid * 16, WS_Y + chunk * 2048, AUX_SC1);
  }
}
}

extern "C" void kernel_launch(void* const* d_in, const int* in_sizes, int n_in, void* d_out, int out_size, void* d_ws, size_t ws_size,
                              hipStream_t stream) {
  typedef const unsigned short* P;
  hipLaunchKernelGGL(k_main, dim3(NWG), dim3(NTHR), 0, stream, (P)d_in[0], (P)d_in[1], (P)d_in[2], (P)d_in[3], (P)d_in[4], (P)d_in[6], (P)d_in[7],
                     (P)d_in[8], (P)d_in[9], (unsigned short*)d_out, (unsigned char*)d_ws);
}
```

```cpp
#include <hip/hip_runtime.h>
#include <hip/hip_bf16.h>
#include <cstdint>

namespace {
typedef unsigned v4u __attribute__((ext_vector_type(4)));
typedef unsigned v2u __attribute__((ext_vector_type(2)));
typedef float f32x4 __attribute__((ext_vector_type(4)));
typedef __bf16 bf16x2 __attribute__((ext_vector_type(2)));
#define GAS __attribute__((address_space(1)))
typedef GAS unsigned gu32;
typedef GAS unsigned long long gu64;
#define RLX_AGENT __ATOMIC_RELAXED, __HIP_MEMORY_SCOPE_AGENT

constexpr int NB = 4, DM = 512, HQ = 32, SK = 4096, DH = 128, FF = 1376;
constexpr int NWG = 256, NW = 8, NTHR = 64 * (NW + 1);
constexpr int NSL = 43;
constexpr int RSW3 = 0x00020000;
constexpr int AUX_NT = 2, AUX_SC1 = 16;
constexpr float LOG2E = 1.4426950408889634f;
constexpr float SCALE = 0.08838834764831845f;
constexpr unsigned SENT = 0xFFFFFFFFu;
constexpr unsigned SPIN_CAP = 1u << 18;

#define AUX_WQ 0
#define AUX_W 0
#define RG 4
#define AUX_K 2
#define AUX_V 2
#define PRIO_MATH 0
#define PACE 0
#define STAGGER 1
#define NOMLP 0
#define ZQ 0
#define ZO 0
#define SLACK 1
#define ROTM 0
#define MAPM 0
#define MCONT 0
#define ZM 0
#define AUX_M 2
#define K0 4

constexpr int WS_R = 0;
constexpr int WS_E = 4096;
constexpr int WS_CONF = 8192;
constexpr int WS_ML = 12288;
constexpr int WS_Y = 16384;
constexpr int WS_YM = WS_Y + 256 * 2048;
constexpr int WS_GU = WS_YM + NSL * 8192;
constexpr int WS_END = WS_GU + 22 * 4096;

__device__ __forceinline__ float dot2(unsigned a, unsigned b, float acc) {
  return __builtin_amdgcn_fdot2_f32_bf16(__builtin_bit_cast(bf16x2, a), __builtin_bit_cast(bf16x2, b), acc, false);
}
__device__ __forceinline__ float bflo(unsigned u) { return __builtin_bit_cast(float, u << 16); }
__device__ __forceinline__ float bfhi(unsigned u) { return __builtin_bit_cast(float, u & 0xffff0000u); }
__device__ __forceinline__ unsigned pk_bf16(float lo, float hi) {
  bf16x2 r; r.x = (__bf16)lo; r.y = (__bf16)hi; return __builtin_bit_cast(unsigned, r);
}
template <int CTRL> __device__ __forceinline__ float dpp(float x) {
  return __builtin_bit_cast(float, __builtin_amdgcn_mov_dpp(__builtin_bit_cast(int, x), CTRL, 0xf, 0xf, true));
}
constexpr int XOR1 = 0xB1, XOR2 = 0x4E, XOR7 = 0x141, XOR8 = 0x128, ROR4 = 0x124;
__device__ __forceinline__ float xrow_sum(float x) {
  auto s = __builtin_amdgcn_permlane16_swap(__float_as_uint(x), __float_as_uint(x), false, false);
  x = __uint_as_float(s[0]) + __uint_as_float(s[1]);
  auto t = __builtin_amdgcn_permlane32_swap(__float_as_uint(x), __float_as_uint(x), false, false);
  return __uint_as_float(t[0]) + __uint_as_float(t[1]);
}
__device__ __forceinline__ float xrow_max(float x) {
  auto s = __builtin_amdgcn_permlane16_swap(__float_as_uint(x), __float_as_uint(x), false, false);
  x = fmaxf(__uint_as_float(s[0]), __uint_as_float(s[1]));
  auto t = __builtin_amdgcn_permlane32_swap(__float_as_uint(x), __float_as_uint(x), false, false);
  return fmaxf(__uint_as_float(t[0]), __uint_as_float(t[1]));
}
__device__ __forceinline__ float row_sum(float x) { x += dpp<XOR1>(x); x += dpp<XOR2>(x); x += dpp<XOR7>(x); x += dpp<XOR8>(x); return x; }
__device__ __forceinline__ float row_max(float x) { x = fmaxf(x, dpp<XOR1>(x)); x = fmaxf(x, dpp<XOR2>(x)); x = fmaxf(x, dpp<XOR7>(x)); x = fmaxf(x, dpp<XOR8>(x)); return x; }
__device__ __forceinline__ float sigmoidf_(float x) { return 1.0f / (1.0f + __expf(-x)); }
__device__ __forceinline__ unsigned canon(float v) { const unsigned u = __float_as_uint(v); return u == SENT ? 0x7FC00000u : u; }
#define SB() __builtin_amdgcn_sched_barrier(0)
#define POLL_FENCE() asm volatile("" ::: "memory")
#define DRAIN() asm volatile("s_waitcnt vmcnt(0)" ::: "memory")
#define BAR() do { asm volatile("s_waitcnt lgkmcnt(0)" ::: "memory"); __builtin_amdgcn_s_barrier(); asm volatile("" ::: "memory"); } while (0)
#define KEEP8(a) asm volatile("" : "+v"(a[0]), "+v"(a[1]), "+v"(a[2]), "+v"(a[3]), "+v"(a[4]), "+v"(a[5]), "+v"(a[6]), "+v"(a[7]))
#define STAMP(k) do { } while (0)

constexpr int L_QP = 0;
constexpr int L_P = L_QP + 8 * 128;
constexpr int L_M = L_P + 8 * 256;
constexpr int L_L = L_M + 8;
constexpr int L_SC = L_L + 8;
constexpr int L_F = L_SC + 8;
constexpr int L_OP = L_F + 8;
constexpr int L_YP = L_OP + 8 * 128;
constexpr int L_XT = L_YP + 8 * 512;
constexpr int L_END = L_XT + 272 * 4;
typedef __attribute__((address_space(3))) unsigned lu32;
__device__ __forceinline__ void lds_arrive(float* p) { asm volatile("s_waitcnt lgkmcnt(0)" ::: "memory"); __hip_atomic_fetch_add((lu32*)p, 1u, __ATOMIC_RELAXED, __HIP_MEMORY_SCOPE_WORKGROUP); }
__device__ __forceinline__ void lds_wait(float* p, unsigned n) {
#pragma unroll 1
  for (unsigned spin = 0; spin < (SPIN_CAP << 4); ++spin) {
    if (__builtin_amdgcn_readfirstlane(__hip_atomic_load((lu32*)p, __ATOMIC_RELAXED, __HIP_MEMORY_SCOPE_WORKGROUP)) >= n) break;
    __builtin_amdgcn_s_sleep(1);
  }
  asm volatile("" ::: "memory");
}

__global__ void __launch_bounds__(NTHR, 3)
k_main(const unsigned short* __restrict__ x, const unsigned short* __restrict__ kc, const unsigned short* __restrict__ vc,
       const unsigned short* __restrict__ wq, const unsigned short* __restrict__ wo, const unsigned short* __restrict__ wg,
       const unsigned short* __restrict__ wu, const unsigned short* __restrict__ wd, const unsigned short* __restrict__ gw,
       unsigned short* __restrict__ out, unsigned char* __restrict__ ws) {
  unsigned long long t_start;
  asm volatile("s_memrealtime %0\n\ts_waitcnt lgkmcnt(0)" : "=s"(t_start) :: "memory");
  __builtin_amdgcn_sched_barrier(0);
  __shared__ __attribute__((aligned(16))) float lds[L_END];
  const int tid = threadIdx.x, lane = tid & 63, w = __builtin_amdgcn_readfirstlane(tid >> 6), r = lane >> 4, c = lane & 15;
  const int bid = blockIdx.x, xg = bid & 7, j = bid >> 3;
  const int h = xg * 4 + (j >> 3), b = (j >> 1) & 3, s = j & 1, m8 = j & 7;
  const int chunk = (b * HQ + h) * 2 + s;
  const __amdgpu_buffer_rsrc_t rx = __builtin_amdgcn_make_buffer_rsrc((void*)x, 0, NB * DM * 2, RSW3);
  const __amdgpu_buffer_rsrc_t rw = __builtin_amdgcn_make_buffer_rsrc((void*)ws, 0, WS_END, RSW3);
  STAMP(0);

  if (w == NW) {
    gu32* R = (gu32*)(ws + WS_R); gu32* E = (gu32*)(ws + WS_E); gu64* ML = (gu64*)(ws + WS_ML);
    __builtin_amdgcn_s_setprio(3);
    if (lane < 3) ((unsigned*)lds)[L_SC + 1 + lane] = 0u;
    const int rho = NOMLP ? 999 : (bid & 1) ? (bid < 8 ? 127 + (bid >> 1) : 999) : (bid >> 1) - 1;
    const bool gu = (unsigned)rho < 88u, dn = rho >= 88 && rho < 88 + NSL;
    const int sl = rho - 88;
    const unsigned r_old = __hip_atomic_load(R + chunk, RLX_AGENT), e_old = __hip_atomic_load(E + chunk, RLX_AGENT);
    v4u rfirst = __builtin_amdgcn_raw_buffer_load_b128(rw, lane * 16, bid == 0 ? WS_R : 0x40000000, AUX_SC1);
    v4u xv[4];
#pragma unroll
    for (int bb = 0; bb < 4; ++bb) xv[bb] = __builtin_amdgcn_raw_buffer_load_b128(rx, lane * 16, bb * DM * 2, 0);
    const v4u gv = *(const GAS v4u*)((const GAS unsigned char*)gw + lane * 16);
    BAR();
    BAR();
    const v4u sent4 = {SENT, SENT, SENT, SENT};
    __builtin_amdgcn_raw_buffer_store_b128(sent4, rw, lane * 16, WS_Y + chunk * 2048, AUX_SC1);
    __builtin_amdgcn_raw_buffer_store_b128(sent4, rw, lane * 16, WS_Y + chunk * 2048 + 1024, AUX_SC1);
    if (dn) {
#pragma unroll 1
      for (int i = 0; i < 8; ++i) __builtin_amdgcn_raw_buffer_store_b128(sent4, rw, lane * 16, WS_YM + sl * 8192 + i * 1024, AUX_SC1);
    }
    if (gu) __builtin_amdgcn_raw_buffer_store_b128(sent4, rw, lane * 16, WS_GU + rho * 1024, AUX_SC1);
    if (lane == 0) __hip_atomic_store(ML + chunk, ~0ull, RLX_AGENT);
    unsigned nonce = (r_old & ~3u) + 4u; if (e_old - nonce < 4u) nonce += 4u;
    float gate[4];
#pragma unroll
    for (int bb = 0; bb < 4; ++bb) {
      float gd = dot2(xv[bb][0], gv[0], 0.f); gd = dot2(xv[bb][1], gv[1], gd); gd = dot2(xv[bb][2], gv[2], gd); gd = dot2(xv[bb][3], gv[3], gd);
      gate[bb] = sigmoidf_(xrow_sum(row_sum(gd)));
    }
    const float gate_b = b == 0 ? gate[0] : b == 1 ? gate[1] : b == 2 ? gate[2] : gate[3];
#pragma unroll
    for (int k = 0; k < 4; ++k) *(v4u*)&lds[L_XT + (4 * lane + k) * 4] = v4u{xv[0][k], xv[1][k], xv[2][k], xv[3][k]};
    DRAIN();
    if (lane == 0) __hip_atomic_store(R + chunk, nonce, RLX_AGENT);
    const __amdgpu_buffer_rsrc_t rm = __builtin_amdgcn_make_buffer_rsrc((void*)(dn ? wd : (rho & 2) ? wu : wg), 0, ((gu || dn) && !ZM) ? DM * FF * 2 : 0, RSW3);
    const int rowb = (dn || MCONT) ? DM * 2 : FF * 2;
    const int mvo = (dn || MCONT) ? lane * 16 : (lane >> 3) * 2 * (FF * 2) + (lane & 7) * 16;
    const int mso = dn ? sl * 32 * (DM * 2) : MCONT ? ((rho >> 2) * 2 + (rho & 1)) * 32000 : (rho & 1) * 256 * (FF * 2) + (rho >> 2) * 128, istep = (dn || MCONT) ? 2 * (DM * 2) : 16 * (FF * 2);
    v4u m0[8], m1[8];
    if (gu || dn) {
#pragma unroll
      for (int i = 0; i < 8; ++i) {
        m0[i] = __builtin_amdgcn_raw_buffer_load_b128(rm, mvo, mso + i * istep, AUX_M); SB();
        m1[i] = __builtin_amdgcn_raw_buffer_load_b128(rm, mvo, mso + i * istep + rowb, AUX_M); SB();
      }
    }
    if (bid == 0) {
      unsigned seen = 0;
#pragma unroll 1
      for (unsigned spin = 0; spin < SPIN_CAP; ++spin) {
        POLL_FENCE();
        __builtin_amdgcn_s_sleep(40);
        v4u rq = __builtin_amdgcn_raw_buffer_load_b128(rw, lane * 16, WS_R, AUX_SC1);
#pragma unroll
        for (int k = 0; k < 4; ++k) seen |= (rq[k] != rfirst[k]) ? 1u << k : 0u;
        if (__all(seen == 15u)) {
          __builtin_amdgcn_raw_buffer_store_b128((rq & ~3u) + 2u, rw, lane * 16, WS_E, AUX_SC1);
          break;
        }
        if (spin >= 2) {
#pragma unroll
          for (int k = 0; k < 4; ++k) if (!(seen >> k & 1u)) __hip_atomic_store(E + lane * 4 + k, rq[k], RLX_AGENT);
        }
      }
    }
    STAMP(4);
#define WAIT_GO() do { _Pragma("unroll 1") for (unsigned spin = 0; spin < SPIN_CAP; ++spin) { POLL_FENCE(); \
        const unsigned e_ = __builtin_amdgcn_readfirstlane(__hip_atomic_load(E + chunk, RLX_AGENT)); \
        if (e_ == nonce + 2u) break; \
        if (e_ == nonce && lane == 0) __hip_atomic_store(R + chunk, nonce + 1u, RLX_AGENT); \
        __builtin_amdgcn_s_sleep(32); } } while (0)
    if (gu || dn) {
      if (dn) {
        lds_wait(&lds[L_SC + 3], 3);
        WAIT_GO();
        v2u pv[4];
        const int go = WS_GU + (sl >> 1) * 4096 + (sl & 1) * 128, gvo = r * 256 + c * 8;
#pragma unroll 1
        for (unsigned spin = 0; spin < SPIN_CAP; ++spin) {
          POLL_FENCE();
          bool ok = true;
#pragma unroll
          for (int k = 0; k < 4; ++k) { pv[k] = __builtin_amdgcn_raw_buffer_load_b64(rw, gvo, go + k * 1024, AUX_SC1); }
#pragma unroll
          for (int k = 0; k < 4; ++k) ok = ok && pv[k][0] != SENT && pv[k][1] != SENT;
          if (__all(ok)) break;
          __builtin_amdgcn_s_sleep(32);
        }
        const float g0 = __uint_as_float(pv[0][0]) + __uint_as_float(pv[1][0]), g1 = __uint_as_float(pv[0][1]) + __uint_as_float(pv[1][1]);
        const float u0 = __uint_as_float(pv[2][0]) + __uint_as_float(pv[3][0]), u1 = __uint_as_float(pv[2][1]) + __uint_as_float(pv[3][1]);
        ((unsigned*)lds)[L_XT + (256 + c) * 4 + r] = pk_bf16(sigmoidf_(g0) * u0, sigmoidf_(g1) * u1);
        asm volatile("s_waitcnt lgkmcnt(0)" ::: "memory");
      }
      const float* xt = &lds[L_XT + (dn ? 256 * 4 : ((rho & 1) * 128 + (lane >> 3)) * 4)];
      const int xstep = dn ? 4 : 32;
      float acc[4][8];
#pragma unroll
      for (int bb = 0; bb < 4; ++bb)
#pragma unroll
        for (int k = 0; k < 8; ++k) acc[bb][k] = 0.f;
#pragma unroll 1
      for (int I0 = 0; I0 < 16; I0 += 8) {
#pragma unroll
        for (int i = 0; i < 8; ++i) {
          __builtin_amdgcn_s_setprio(PRIO_MATH);
          const v4u xp = *(const v4u*)(xt + (I0 + i) * xstep);
#pragma unroll
          for (int k = 0; k < 4; ++k) {
            const unsigned lo = __builtin_amdgcn_perm(m1[i][k], m0[i][k], 0x05040100u), hi = __builtin_amdgcn_perm(m1[i][k], m0[i][k], 0x07060302u);
#pragma unroll
            for (int bb = 0; bb < 4; ++bb) { acc[bb][2 * k] = dot2(lo, xp[bb], acc[bb][2 * k]); acc[bb][2 * k + 1] = dot2(hi, xp[bb], acc[bb][2 * k + 1]); }
          }
          KEEP8(acc[0]); KEEP8(acc[1]); KEEP8(acc[2]); KEEP8(acc[3]);
          SB();
          __builtin_amdgcn_s_setprio(3);
          const int o = I0 == 0 ? mso + (8 + i) * istep : 0x40000000;
          m0[i] = __builtin_amdgcn_raw_buffer_load_b128(rm, mvo, o, AUX_M); SB();
          m1[i] = __builtin_amdgcn_raw_buffer_load_b128(rm, mvo, o + rowb, AUX_M); SB();
        }
      }
      if (!dn) {
#pragma unroll
        for (int bb = 0; bb < 4; ++bb) {
          *(f32x4*)&lds[L_YP + lane * 32 + bb * 8] = f32x4{acc[bb][0], acc[bb][1], acc[bb][2], acc[bb][3]};
          *(f32x4*)&lds[L_YP + lane * 32 + bb * 8 + 4] = f32x4{acc[bb][4], acc[bb][5], acc[bb][6], acc[bb][7]};
        }
        asm volatile("s_waitcnt lgkmcnt(0)" ::: "memory");
        f32x4 g4 = {0.f, 0.f, 0.f, 0.f};
        const float* sp = &lds[L_YP + (c >> 1) * 32 + r * 8 + (c & 1) * 4];
#pragma unroll
        for (int rg = 0; rg < 8; ++rg) g4 += *(const f32x4*)(sp + rg * 256);
        __builtin_amdgcn_raw_buffer_store_b128(v4u{canon(g4[0]), canon(g4[1]), canon(g4[2]), canon(g4[3])}, rw, lane * 16, WS_GU + rho * 1024, AUX_SC1);
      } else {
#pragma unroll
        for (int bb = 0; bb < 4; ++bb) {
          const float f = 1.0f - gate[bb];
          const v4u y0 = {canon(acc[bb][0] * f), canon(acc[bb][1] * f), canon(acc[bb][2] * f), canon(acc[bb][3] * f)};
          const v4u y1 = {canon(acc[bb][4] * f), canon(acc[bb][5] * f), canon(acc[bb][6] * f), canon(acc[bb][7] * f)};
          __builtin_amdgcn_raw_buffer_store_b128(y0, rw, lane * 32, WS_YM + sl * 8192 + bb * 2048, AUX_SC1);
          __builtin_amdgcn_raw_buffer_store_b128(y1, rw, lane * 32 + 16, WS_YM + sl * 8192 + bb * 2048, AUX_SC1);
        }
      }
    }
    STAMP(1);
    lds_wait(&lds[L_SC + 2], NW);
    const float mw = lds[L_M + (lane & 7)];
    float M = fmaxf(mw, dpp<XOR1>(mw)); M = fmaxf(M, dpp<XOR2>(M)); M = fmaxf(M, dpp<XOR7>(M));
    const float ew = __builtin_amdgcn_exp2f(mw - M);
    float L = ew * lds[L_L + (lane & 7)]; L += dpp<XOR1>(L); L += dpp<XOR2>(L); L += dpp<XOR7>(L);
    if (lane == 0) __hip_atomic_store(ML + chunk, ((unsigned long long)canon(L) << 32) | canon(M), RLX_AGENT);
    if (!dn) WAIT_GO();
    lds_wait(&lds[L_SC + 3], 6);
    unsigned long long pm = ~0ull;
#pragma unroll 1
    for (unsigned spin = 0; spin < SPIN_CAP; ++spin) {
      POLL_FENCE();
      pm = __hip_atomic_load(ML + (chunk ^ 1), RLX_AGENT);
      if (__builtin_amdgcn_readfirstlane((unsigned)pm) != SENT) break;
      __builtin_amdgcn_s_sleep(32);
    }
    {
      const float M2 = __uint_as_float((unsigned)pm), L2 = __uint_as_float((unsigned)(pm >> 32));
      const float Mx = fmaxf(M, M2), e1 = __builtin_amdgcn_exp2f(M - Mx), e2 = __builtin_amdgcn_exp2f(M2 - Mx);
      if (lane < 8) lds[L_F + lane] = ew * gate_b * e1 / (e1 * L + e2 * L2);
    }
    STAMP(2);
    const int u = chunk & 63;
    float xs[8];
#pragma unroll
    for (int k = 0; k < 8; ++k) xs[k] = 0.f;
#pragma unroll 1
    for (int round = 0; round < 2; ++round) {
      const int yo = round ? WS_Y + (b * 64) * 2048 + u * 32 : WS_YM + b * 2048 + u * 32;
      const int vo = round ? lane * 2048 : lane * 8192;
      v4u y0 = {0u, 0u, 0u, 0u}, y1 = y0;
      bool ok = !round && (NOMLP || lane >= NSL);
      BAR();
      if (round) { STAMP(3); }
#pragma unroll 1
      for (unsigned spin = 0; spin < SPIN_CAP; ++spin) {
        POLL_FENCE();
        if (!ok) {
          y0 = __builtin_amdgcn_raw_buffer_load_b128(rw, vo, yo, AUX_SC1);
          y1 = __builtin_amdgcn_raw_buffer_load_b128(rw, vo, yo + 16, AUX_SC1);
          ok = y0[0] != SENT && y0[1] != SENT && y0[2] != SENT && y0[3] != SENT && y1[0] != SENT && y1[1] != SENT && y1[2] != SENT && y1[3] != SENT;
        }
        if (__all(ok)) break;
        __builtin_amdgcn_s_sleep(2);
      }
#pragma unroll
      for (int k = 0; k < 4; ++k) { xs[k] += __uint_as_float(y0[k]); xs[4 + k] += __uint_as_float(y1[k]); }
    }
#define TR_STEP(HALF, CTRL, BIT) _Pragma("unroll") for (int hh = 0; hh < (HALF); ++hh) { \
      const float lo_ = xs[hh], hi_ = xs[hh + (HALF)]; const float t1_ = lo_ + dpp<CTRL>(lo_), t2_ = hi_ + dpp<CTRL>(hi_); \
      xs[hh] = (c & (BIT)) ? t2_ : t1_; }
    TR_STEP(4, XOR8, 8) TR_STEP(2, XOR7, 4) TR_STEP(1, XOR2, 2)
    float o = xs[0]; o += dpp<XOR1>(o); o = xrow_sum(o);
    while (__builtin_amdgcn_s_memrealtime() - t_start < 7000ull) __builtin_amdgcn_s_sleep(1);
    if (lane < 16 && !(lane & 1)) out[b * DM + u * 8 + (lane >> 1)] = __builtin_bit_cast(unsigned short, (__bf16)o);
    STAMP(7);
    return;
  }

  const __amdgpu_buffer_rsrc_t rk = __builtin_amdgcn_make_buffer_rsrc((void*)kc, 0, NB * HQ * SK * DH * 2, RSW3);
  const __amdgpu_buffer_rsrc_t rv = __builtin_amdgcn_make_buffer_rsrc((void*)vc, 0, NB * HQ * SK * DH * 2, RSW3);
  const __amdgpu_buffer_rsrc_t rq = __builtin_amdgcn_make_buffer_rsrc((void*)wq, 0, ZQ ? 0 : DM * HQ * DH * 2, RSW3);
  const __amdgpu_buffer_rsrc_t ro = __builtin_amdgcn_make_buffer_rsrc((void*)wo, 0, ZO ? 0 : DM * HQ * DH * 2, RSW3);

  const int kvoff = lane * 16;
  const int kvbase = chunk * (512 * 1024) + w * 4096;
  const int r0 = ROTM == 0 ? chunk * 4 : ROTM == 14 ? chunk * 4 + 4 * (chunk >> 3) : ROTM == 12 ? chunk * 4 + 4 * (chunk >> 4) : chunk * 4 + 4 * (chunk >> 6);
#define KVOFF(t) (kvbase + ((((t) + r0) & 15) << 15))
  v4u wq_[16], wt[16];
  v4u a[4 * RG];
  unsigned xd[8];
  const int oso = (h * DH + 16 * w) * (DM * 2);
  const int qvoff = r * 2 * (HQ * DH * 2) + c * 16, qso = 8 * w * (HQ * DH * 2) + h * DH * 2;
  const int rot = STAGGER ? 2 * b : m8;
#define KLOAD(n) { a[n] = __builtin_amdgcn_raw_buffer_load_b128(rk, kvoff, KVOFF((n) >> 2) + ((n) & 3) * 1024, AUX_K); SB(); }
#define LD_O(i) { wt[i] = __builtin_amdgcn_raw_buffer_load_b128(ro, kvoff, oso + (((i) + 2 * m8) & 15) * (DM * 2), AUX_W); SB(); }
#define LD_Q(i) { wq_[i] = __builtin_amdgcn_raw_buffer_load_b128(rq, qvoff, qso + (64 * ((((i) >> 1) + rot) & 7) + ((i) & 1)) * (HQ * DH * 2), AUX_WQ); SB(); }
#define LD_X() { _Pragma("unroll") for (int k = 0; k < 8; ++k) xd[k] = __builtin_amdgcn_raw_buffer_load_b32(rx, r * 4, (b * DM + 64 * ((k + rot) & 7) + 8 * w) * 2, 0); SB(); }
#define QPROJ() { \
    float qa[8]; \
    _Pragma("unroll") for (int k = 0; k < 8; ++k) qa[k] = 0.f; \
    _Pragma("unroll") for (int i = 0; i < 16; ++i) { \
      const unsigned xx = xd[i >> 1]; \
      const unsigned xl = (i & 1) ? (xx >> 16) : (xx & 0xffffu), xh = (i & 1) ? (xx & 0xffff0000u) : (xx << 16); \
      _Pragma("unroll") for (int k = 0; k < 4; ++k) { qa[2 * k] = dot2(wq_[i][k], xl, qa[2 * k]); qa[2 * k + 1] = dot2(wq_[i][k], xh, qa[2 * k + 1]); } \
    } \
    _Pragma("unroll") for (int k = 0; k < 8; ++k) qa[k] = xrow_sum(qa[k]); \
    KEEP8(qa); \
    if (r == 0) { \
      *(f32x4*)&lds[L_QP + w * 128 + c * 8] = f32x4{qa[0], qa[1], qa[2], qa[3]}; \
      *(f32x4*)&lds[L_QP + w * 128 + c * 8 + 4] = f32x4{qa[4], qa[5], qa[6], qa[7]}; \
    } \
    SB(); }
  if (!STAGGER || s == 0) {
#pragma unroll
    for (int i = 0; i < 4; ++i) LD_Q(i)
    LD_X()
#pragma unroll
    for (int i = 4; i < 16; ++i) LD_Q(i)
    BAR();
    SB();
    STAMP(1);
#pragma unroll
    for (int n = 0; n < K0; ++n) KLOAD(n)
    QPROJ()
#pragma unroll
    for (int n = K0; n < 4 * RG; ++n) KLOAD(n)
  } else {
#pragma unroll
    for (int n = 0; n < 4 * RG; ++n) KLOAD(n)
    BAR();
    SB();
    STAMP(1);
    LD_X()
#pragma unroll
    for (int i = 0; i < 16; ++i) LD_Q(i)
    QPROJ()
  }
  BAR();
  STAMP(2);
  unsigned qpk[4];
  {
    f32x4 q0 = {0.f, 0.f, 0.f, 0.f}, q1 = q0;
#pragma unroll
    for (int ww = 0; ww < NW; ++ww) { q0 += *(const f32x4*)&lds[L_QP + ww * 128 + c * 8]; q1 += *(const f32x4*)&lds[L_QP + ww * 128 + c * 8 + 4]; }
    qpk[0] = pk_bf16(q0[0], q0[1]); qpk[1] = pk_bf16(q0[2], q0[3]); qpk[2] = pk_bf16(q1[0], q1[1]); qpk[3] = pk_bf16(q1[2], q1[3]);
    asm volatile("" : "+v"(qpk[0]), "+v"(qpk[1]), "+v"(qpk[2]), "+v"(qpk[3]));
    SB();
  }
  STAMP(3);
  float* prow = &lds[L_P + (w * 4 + r) * 64];
  float mx = -INFINITY;
#define PACE_IN(st) { if (SLACK && (st) >= SLACK) lds_wait(&lds[L_SC + 1], NW * ((st) - SLACK + 1)); }
#define PACE_OUT() { if (SLACK && lane == 0) __hip_atomic_fetch_add((lu32*)&lds[L_SC + 1], 1u, __ATOMIC_RELAXED, __HIP_MEMORY_SCOPE_WORKGROUP); }
#define KSTEP(T, RN, AUXN) { \
    PACE_IN(T) \
    float xs[16]; \
    _Pragma("unroll") for (int g = 0; g < 4; ++g) { \
      const int o = KVOFF(4 * (T) + g + RG); \
      _Pragma("unroll") for (int i = 0; i < 4; ++i) { \
        v4u& kk = a[(g % RG) * 4 + i]; \
        float d = dot2(kk[0], qpk[0], 0.f); d = dot2(kk[1], qpk[1], d); d = dot2(kk[2], qpk[2], d); d = dot2(kk[3], qpk[3], d); \
        asm volatile("" : "+v"(d)); \
        xs[g * 4 + i] = d; \
        SB(); \
        kk = __builtin_amdgcn_raw_buffer_load_b128(RN, kvoff, o + i * 1024, AUXN); \
        SB(); \
      } \
      LD_O(4 * (T) + g) \
    } \
    PACE_OUT() \
    TR_STEP(8, XOR8, 8) TR_STEP(4, XOR7, 4) TR_STEP(2, XOR2, 2) TR_STEP(1, XOR1, 1) \
    const float sc = xs[0] * (SCALE * LOG2E); \
    prow[16 * (T) + c] = sc; \
    mx = fmaxf(mx, sc); \
    if (tid == 64 * (NW - 1)) __hip_atomic_store((lu32*)&lds[L_SC + 3], (unsigned)(T) + 1u, __ATOMIC_RELAXED, __HIP_MEMORY_SCOPE_WORKGROUP); }
  static_assert(RG == 4, "the last K step refills the whole ring from V");
  KSTEP(0, rk, AUX_K) KSTEP(1, rk, AUX_K) KSTEP(2, rk, AUX_K) KSTEP(3, rv, AUX_V)
  STAMP(4);
  mx = xrow_max(row_max(mx));
  {
    float ls = 0.f;
#pragma unroll
    for (int T = 0; T < 4; ++T) { const float p = __builtin_amdgcn_exp2f(prow[16 * T + c] - mx); prow[16 * T + c] = p; ls += p; }
    ls = xrow_sum(row_sum(ls));
    if (lane == 0) { lds[L_M + w] = mx; lds[L_L + w] = ls; lds_arrive(&lds[L_SC + 2]); }
  }
  asm volatile("s_waitcnt lgkmcnt(0)" ::: "memory");
  SB();
  STAMP(5);
#define WPERM(p) { _Pragma("unroll") for (int k = 0; k < 4; ++k) { const unsigned lo_ = __builtin_amdgcn_perm(wt[2 * (p) + 1][k], wt[2 * (p)][k], 0x05040100u), \
      hi_ = __builtin_amdgcn_perm(wt[2 * (p) + 1][k], wt[2 * (p)][k], 0x07060302u); wt[2 * (p)][k] = lo_; wt[2 * (p) + 1][k] = hi_; } \
      asm volatile("" : "+v"(wt[2 * (p)]), "+v"(wt[2 * (p) + 1])); }
  WPERM(0) WPERM(1) WPERM(2) WPERM(3) WPERM(4) WPERM(5)
  SB();
  float oa[8];
#pragma unroll
  for (int k = 0; k < 8; ++k) oa[k] = 0.f;
#pragma unroll 1
  for (int T = 0; T < 4; ++T) {
    if (!SLACK && T == PACE) lds_wait(&lds[L_SC + 2], NW);
    PACE_IN(4 + T)
#pragma unroll
    for (int g = 0; g < 4; ++g) {
      const int un = 4 * T + g + RG;
      const int o = un < 16 ? KVOFF(un) : 0x40000000;
      const f32x4 p4 = *(const f32x4*)&prow[16 * T + 4 * g];
#pragma unroll
      for (int i = 0; i < 4; ++i) {
        v4u& vv = a[(g % RG) * 4 + i];
#pragma unroll
        for (int k = 0; k < 4; ++k) { oa[2 * k] = fmaf(p4[i], bflo(vv[k]), oa[2 * k]); oa[2 * k + 1] = fmaf(p4[i], bfhi(vv[k]), oa[2 * k + 1]); }
        KEEP8(oa);
        SB();
        vv = __builtin_amdgcn_raw_buffer_load_b128(rv, kvoff, o + i * 1024, AUX_V);
        SB();
      }
    }
    PACE_OUT()
    if (tid == 64 * (NW - 1)) __hip_atomic_store((lu32*)&lds[L_SC + 3], (unsigned)T + 5u, __ATOMIC_RELAXED, __HIP_MEMORY_SCOPE_WORKGROUP);
  }
  WPERM(6) WPERM(7)
#pragma unroll
  for (int k = 0; k < 8; ++k) oa[k] = xrow_sum(oa[k]);
  STAMP(6);
  if (r == 0) {
    *(f32x4*)&lds[L_OP + w * 128 + c * 8] = f32x4{oa[0], oa[1], oa[2], oa[3]};
    *(f32x4*)&lds[L_OP + w * 128 + c * 8 + 4] = f32x4{oa[4], oa[5], oa[6], oa[7]};
  }
  BAR();
  float od = 0.f;
#pragma unroll
  for (int ww = 0; ww < NW; ++ww) od = fmaf(lds[L_F + ww], lds[L_OP + ww * 128 + 16 * w + ((c + 2 * m8) & 15)], od);
  const unsigned opk = pk_bf16(od, dpp<XOR1>(od));
  float ya[8];
#pragma unroll
  for (int k = 0; k < 8; ++k) ya[k] = 0.f;
#pragma unroll
  for (int p = 0; p < 8; ++p) {
    const unsigned ov = __builtin_amdgcn_readlane(opk, 2 * p);
#pragma unroll
    for (int k = 0; k < 4; ++k) { ya[2 * k] = dot2(wt[2 * p][k], ov, ya[2 * k]); ya[2 * k + 1] = dot2(wt[2 * p + 1][k], ov, ya[2 * k + 1]); }
  }
  *(f32x4*)&lds[L_YP + w * 512 + lane * 8] = f32x4{ya[0], ya[1], ya[2], ya[3]};
  *(f32x4*)&lds[L_YP + w * 512 + lane * 8 + 4] = f32x4{ya[4], ya[5], ya[6], ya[7]};
  BAR();
  if (w < 2) {
    f32x4 y = *(const f32x4*)&lds[L_YP + tid * 4];
#pragma unroll
    for (int ww = 1; ww < NW; ++ww) y += *(const f32x4*)&lds[L_YP + ww * 512 + tid * 4];
    const v4u yu = {canon(y[0]), canon(y[1]), canon(y[2]), canon(y[3])};
    __builtin_amdgcn_raw_buffer_store_b128(yu, rw, tid * 16, WS_Y + chunk * 2048, AUX_SC1);
  }
}
}

extern "C" void kernel_launch(void* const* d_in, const int* in_sizes, int n_in, void* d_out, int out_size, void* d_ws, size_t ws_size,
                              hipStream_t stream) {
  typedef const unsigned short* P;
  hipLaunchKernelGGL(k_main, dim3(NWG), dim3(NTHR), 0, stream, (P)d_in[0], (P)d_in[1], (P)d_in[2], (P)d_in[3], (P)d_in[4], (P)d_in[6], (P)d_in[7],
                     (P)d_in[8], (P)d_in[9], (unsigned short*)d_out, (unsigned char*)d_ws);
}
```

```cpp
#include <hip/hip_runtime.h>
#include <hip/hip_bf16.h>
#include <cstdint>

namespace {
typedef unsigned v4u __attribute__((ext_vector_type(4)));
typedef unsigned v2u __attribute__((ext_vector_type(2)));
typedef float f32x4 __attribute__((ext_vector_type(4)));
typedef __bf16 bf16x2 __attribute__((ext_vector_type(2)));
#define GAS __attribute__((address_space(1)))
typedef GAS unsigned gu32;
typedef GAS unsigned long long gu64;
#define RLX_AGENT __ATOMIC_RELAXED, __HIP_MEMORY_SCOPE_AGENT

constexpr int NB = 4, DM = 512, HQ = 32, SK = 4096, DH = 128, FF = 1376;
constexpr int NWG = 256, NW = 8, NTHR = 64 * (NW + 1);
constexpr int NSL = 43;
constexpr int RSW3 = 0x00020000;
constexpr int AUX_NT = 2, AUX_SC1 = 16;
constexpr float LOG2E = 1.4426950408889634f;
constexpr float SCALE = 0.08838834764831845f;
constexpr unsigned SENT = 0xFFFFFFFFu;
constexpr unsigned SPIN_CAP = 1u << 18;

#define AUX_WQ 0
#define AUX_W 0
#define RG 4
#define AUX_K 2
#define AUX_V 2
#define PRIO_MATH 0
#define PACE 0
#define STAGGER 1
#define NOMLP 0
#define ZQ 0
#define ZO 0
#define SLACK 1
#define ROTM 0
#define MAPM 0
#define MCONT 0
#define ZM 0
#define AUX_M 2
#define POLL_GAP 4
#define K0 4

constexpr int WS_R = 0;
constexpr int WS_E = 4096;
constexpr int WS_CONF = 8192;
constexpr int WS_ML = 12288;
constexpr int WS_Y = 16384;
constexpr int WS_YM = WS_Y + 256 * 2048;
constexpr int WS_GU = WS_YM + NSL * 8192;
constexpr int WS_END = WS_GU + 22 * 4096;

__device__ __forceinline__ float dot2(unsigned a, unsigned b, float acc) {
  return __builtin_amdgcn_fdot2_f32_bf16(__builtin_bit_cast(bf16x2, a), __builtin_bit_cast(bf16x2, b), acc, false);
}
__device__ __forceinline__ float bflo(unsigned u) { return __builtin_bit_cast(float, u << 16); }
__device__ __forceinline__ float bfhi(unsigned u) { return __builtin_bit_cast(float, u & 0xffff0000u); }
__device__ __forceinline__ unsigned pk_bf16(float lo, float hi) {
  bf16x2 r; r.x = (__bf16)lo; r.y = (__bf16)hi; return __builtin_bit_cast(unsigned, r);
}
template <int CTRL> __device__ __forceinline__ float dpp(float x) {
  return __builtin_bit_cast(float, __builtin_amdgcn_mov_dpp(__builtin_bit_cast(int, x), CTRL, 0xf, 0xf, true));
}
constexpr int XOR1 = 0xB1, XOR2 = 0x4E, XOR7 = 0x141, XOR8 = 0x128, ROR4 = 0x124;
__device__ __forceinline__ float xrow_sum(float x) {
  auto s = __builtin_amdgcn_permlane16_swap(__float_as_uint(x), __float_as_uint(x), false, false);
  x = __uint_as_float(s[0]) + __uint_as_float(s[1]);
  auto t = __builtin_amdgcn_permlane32_swap(__float_as_uint(x), __float_as_uint(x), false, false);
  return __uint_as_float(t[0]) + __uint_as_float(t[1]);
}
__device__ __forceinline__ float xrow_max(float x) {
  auto s = __builtin_amdgcn_permlane16_swap(__float_as_uint(x), __float_as_uint(x), false, false);
  x = fmaxf(__uint_as_float(s[0]), __uint_as_float(s[1]));
  auto t = __builtin_amdgcn_permlane32_swap(__float_as_uint(x), __float_as_uint(x), false, false);
  return fmaxf(__uint_as_float(t[0]), __uint_as_float(t[1]));
}
__device__ __forceinline__ float row_sum(float x) { x += dpp<XOR1>(x); x += dpp<XOR2>(x); x += dpp<XOR7>(x); x += dpp<XOR8>(x); return x; }
__device__ __forceinline__ float row_max(float x) { x = fmaxf(x, dpp<XOR1>(x)); x = fmaxf(x, dpp<XOR2>(x)); x = fmaxf(x, dpp<XOR7>(x)); x = fmaxf(x, dpp<XOR8>(x)); return x; }
__device__ __forceinline__ float sigmoidf_(float x) { return 1.0f / (1.0f + __expf(-x)); }
__device__ __forceinline__ unsigned canon(float v) { const unsigned u = __float_as_uint(v); return u == SENT ? 0x7FC00000u : u; }
#define SB() __builtin_amdgcn_sched_barrier(0)
#define POLL_FENCE() asm volatile("" ::: "memory")
#define DRAIN() asm volatile("s_waitcnt vmcnt(0)" ::: "memory")
#define BAR() do { asm volatile("s_waitcnt lgkmcnt(0)" ::: "memory"); __builtin_amdgcn_s_barrier(); asm volatile("" ::: "memory"); } while (0)
#define KEEP8(a) asm volatile("" : "+v"(a[0]), "+v"(a[1]), "+v"(a[2]), "+v"(a[3]), "+v"(a[4]), "+v"(a[5]), "+v"(a[6]), "+v"(a[7]))
#define STAMP(k) do { } while (0)

constexpr int L_QP = 0;
constexpr int L_P = L_QP + 8 * 128;
constexpr int L_M = L_P + 8 * 256;
constexpr int L_L = L_M + 8;
constexpr int L_SC = L_L + 8;
constexpr int L_F = L_SC + 8;
constexpr int L_OP = L_F + 8;
constexpr int L_YP = L_OP + 8 * 128;
constexpr int L_XT = L_YP + 8 * 512;
constexpr int L_END = L_XT + 272 * 4;
typedef __attribute__((address_space(3))) unsigned lu32;
__device__ __forceinline__ void lds_arrive(float* p) { asm volatile("s_waitcnt lgkmcnt(0)" ::: "memory"); __hip_atomic_fetch_add((lu32*)p, 1u, __ATOMIC_RELAXED, __HIP_MEMORY_SCOPE_WORKGROUP); }
__device__ __forceinline__ void lds_wait(float* p, unsigned n) {
#pragma unroll 1
  for (unsigned spin = 0; spin < (SPIN_CAP << 4); ++spin) {
    if (__builtin_amdgcn_readfirstlane(__hip_atomic_load((lu32*)p, __ATOMIC_RELAXED, __HIP_MEMORY_SCOPE_WORKGROUP)) >= n) break;
    __builtin_amdgcn_s_sleep(1);
  }
  asm volatile("" ::: "memory");
}

__global__ void __launch_bounds__(NTHR, 3)
k_main(const unsigned short* __restrict__ kc, const unsigned short* __restrict__ wq, const unsigned short* __restrict__ x,
       const unsigned short* __restrict__ vc, const unsigned short* __restrict__ wo, unsigned char* __restrict__ ws,
       const unsigned short* __restrict__ gw, unsigned short* __restrict__ out,
       const unsigned short* __restrict__ wg, const unsigned short* __restrict__ wu, const unsigned short* __restrict__ wd) {
  __shared__ __attribute__((aligned(16))) float lds[L_END];
  asm volatile("" :: "s"(x), "s"(kc), "s"(vc), "s"(wq), "s"(wo), "s"(ws), "s"(gw), "s"(out), "s"(wg), "s"(wu), "s"(wd));
  const int tid = threadIdx.x, lane = tid & 63, w = __builtin_amdgcn_readfirstlane(tid >> 6), r = lane >> 4, c = lane & 15;
  const int bid = blockIdx.x, xg = bid & 7, j = bid >> 3;
  const int h = xg * 4 + (j >> 3), b = (j >> 1) & 3, s = j & 1, m8 = j & 7;
  const int chunk = (b * HQ + h) * 2 + s;
  const __amdgpu_buffer_rsrc_t rx = __builtin_amdgcn_make_buffer_rsrc((void*)x, 0, NB * DM * 2, RSW3);
  const __amdgpu_buffer_rsrc_t rw = __builtin_amdgcn_make_buffer_rsrc((void*)ws, 0, WS_END, RSW3);
  STAMP(0);

  if (w == NW) {
    gu32* R = (gu32*)(ws + WS_R); gu32* E = (gu32*)(ws + WS_E); gu64* ML = (gu64*)(ws + WS_ML);
    __builtin_amdgcn_s_setprio(3);
    if (lane < 3) ((unsigned*)lds)[L_SC + 1 + lane] = 0u;
    const int rho = NOMLP ? 999 : (bid & 1) ? (bid < 8 ? 127 + (bid >> 1) : 999) : (bid >> 1) - 1;
    const bool gu = (unsigned)rho < 88u, dn = rho >= 88 && rho < 88 + NSL;
    const int sl = rho - 88;
    const unsigned r_old = __hip_atomic_load(R + chunk, RLX_AGENT), e_old = __hip_atomic_load(E + chunk, RLX_AGENT);
    v4u rfirst = __builtin_amdgcn_raw_buffer_load_b128(rw, lane * 16, bid == 0 ? WS_R : 0x40000000, AUX_SC1);
    v4u xv[4];
#pragma unroll
    for (int bb = 0; bb < 4; ++bb) xv[bb] = __builtin_amdgcn_raw_buffer_load_b128(rx, lane * 16, bb * DM * 2, 0);
    const v4u gv = *(const GAS v4u*)((const GAS unsigned char*)gw + lane * 16);
    BAR();
    BAR();
    const v4u sent4 = {SENT, SENT, SENT, SENT};
    __builtin_amdgcn_raw_buffer_store_b128(sent4, rw, lane * 16, WS_Y + chunk * 2048, AUX_SC1);
    __builtin_amdgcn_raw_buffer_store_b128(sent4, rw, lane * 16, WS_Y + chunk * 2048 + 1024, AUX_SC1);
    if (dn) {
#pragma unroll 1
      for (int i = 0; i < 8; ++i) __builtin_amdgcn_raw_buffer_store_b128(sent4, rw, lane * 16, WS_YM + sl * 8192 + i * 1024, AUX_SC1);
    }
    if (gu) __builtin_amdgcn_raw_buffer_store_b128(sent4, rw, lane * 16, WS_GU + rho * 1024, AUX_SC1);
    if (lane == 0) __hip_atomic_store(ML + chunk, ~0ull, RLX_AGENT);
    unsigned nonce = (r_old & ~3u) + 4u; if (e_old - nonce < 4u) nonce += 4u;
    float gate[4];
#pragma unroll
    for (int bb = 0; bb < 4; ++bb) {
      float gd = dot2(xv[bb][0], gv[0], 0.f); gd = dot2(xv[bb][1], gv[1], gd); gd = dot2(xv[bb][2], gv[2], gd); gd = dot2(xv[bb][3], gv[3], gd);
      gate[bb] = sigmoidf_(xrow_sum(row_sum(gd)));
    }
    const float gate_b = b == 0 ? gate[0] : b == 1 ? gate[1] : b == 2 ? gate[2] : gate[3];
#pragma unroll
    for (int k = 0; k < 4; ++k) *(v4u*)&lds[L_XT + (4 * lane + k) * 4] = v4u{xv[0][k], xv[1][k], xv[2][k], xv[3][k]};
    DRAIN();
    if (lane == 0) __hip_atomic_store(R + chunk, nonce, RLX_AGENT);
    const __amdgpu_buffer_rsrc_t rm = __builtin_amdgcn_make_buffer_rsrc((void*)(dn ? wd : (rho & 2) ? wu : wg), 0, ((gu || dn) && !ZM) ? DM * FF * 2 : 0, RSW3);
    const int rowb = (dn || MCONT) ? DM * 2 : FF * 2;
    const int mvo = (dn || MCONT) ? lane * 16 : (lane >> 3) * 2 * (FF * 2) + (lane & 7) * 16;
    const int mso = dn ? sl * 32 * (DM * 2) : MCONT ? ((rho >> 2) * 2 + (rho & 1)) * 32000 : (rho & 1) * 256 * (FF * 2) + (rho >> 2) * 128, istep = (dn || MCONT) ? 2 * (DM * 2) : 16 * (FF * 2);
    v4u m0[8], m1[8];
    if (gu || dn) {
#pragma unroll
      for (int i = 0; i < 8; ++i) {
        m0[i] = __builtin_amdgcn_raw_buffer_load_b128(rm, mvo, mso + i * istep, AUX_M); SB();
        m1[i] = __builtin_amdgcn_raw_buffer_load_b128(rm, mvo, mso + i * istep + rowb, AUX_M); SB();
      }
    }
    if (bid == 0) {
      unsigned seen = 0;
#pragma unroll 1
      for (unsigned spin = 0; spin < SPIN_CAP; ++spin) {
        POLL_FENCE();
        __builtin_amdgcn_s_sleep(40);
        v4u rq = __builtin_amdgcn_raw_buffer_load_b128(rw, lane * 16, WS_R, AUX_SC1);
#pragma unroll
        for (int k = 0; k < 4; ++k) seen |= (rq[k] != rfirst[k]) ? 1u << k : 0u;
        if (__all(seen == 15u)) {
          __builtin_amdgcn_raw_buffer_store_b128((rq & ~3u) + 2u, rw, lane * 16, WS_E, AUX_SC1);
          break;
        }
        if (spin >= 2) {
#pragma unroll
          for (int k = 0; k < 4; ++k) if (!(seen >> k & 1u)) __hip_atomic_store(E + lane * 4 + k, rq[k], RLX_AGENT);
        }
      }
    }
    STAMP(4);
#define WAIT_GO() do { _Pragma("unroll 1") for (unsigned spin = 0; spin < SPIN_CAP; ++spin) { POLL_FENCE(); \
        const unsigned e_ = __builtin_amdgcn_readfirstlane(__hip_atomic_load(E + chunk, RLX_AGENT)); \
        if (e_ == nonce + 2u) break; \
        if (e_ == nonce && lane == 0) __hip_atomic_store(R + chunk, nonce + 1u, RLX_AGENT); \
        __builtin_amdgcn_s_sleep(32); } } while (0)
    if (gu || dn) {
      if (dn) {
        lds_wait(&lds[L_SC + 3], 3);
        WAIT_GO();
        v2u pv[4];
        const int go = WS_GU + (sl >> 1) * 4096 + (sl & 1) * 128, gvo = r * 256 + c * 8;
#pragma unroll 1
        for (unsigned spin = 0; spin < SPIN_CAP; ++spin) {
          POLL_FENCE();
          bool ok = true;
#pragma unroll
          for (int k = 0; k < 4; ++k) { pv[k] = __builtin_amdgcn_raw_buffer_load_b64(rw, gvo, go + k * 1024, AUX_SC1); }
#pragma unroll
          for (int k = 0; k < 4; ++k) ok = ok && pv[k][0] != SENT && pv[k][1] != SENT;
          if (__all(ok)) break;
          __builtin_amdgcn_s_sleep(32);
        }
        const float g0 = __uint_as_float(pv[0][0]) + __uint_as_float(pv[1][0]), g1 = __uint_as_float(pv[0][1]) + __uint_as_float(pv[1][1]);
        const float u0 = __uint_as_float(pv[2][0]) + __uint_as_float(pv[3][0]), u1 = __uint_as_float(pv[2][1]) + __uint_as_float(pv[3][1]);
        ((unsigned*)lds)[L_XT + (256 + c) * 4 + r] = pk_bf16(sigmoidf_(g0) * u0, sigmoidf_(g1) * u1);
        asm volatile("s_waitcnt lgkmcnt(0)" ::: "memory");
      }
      const float* xt = &lds[L_XT + (dn ? 256 * 4 : ((rho & 1) * 128 + (lane >> 3)) * 4)];
      const int xstep = dn ? 4 : 32;
      float acc[4][8];
#pragma unroll
      for (int bb = 0; bb < 4; ++bb)
#pragma unroll
        for (int k = 0; k < 8; ++k) acc[bb][k] = 0.f;
#pragma unroll 1
      for (int I0 = 0; I0 < 16; I0 += 8) {
#pragma unroll
        for (int i = 0; i < 8; ++i) {
          __builtin_amdgcn_s_setprio(PRIO_MATH);
          const v4u xp = *(const v4u*)(xt + (I0 + i) * xstep);
#pragma unroll
          for (int k = 0; k < 4; ++k) {
            const unsigned lo = __builtin_amdgcn_perm(m1[i][k], m0[i][k], 0x05040100u), hi = __builtin_amdgcn_perm(m1[i][k], m0[i][k], 0x07060302u);
#pragma unroll
            for (int bb = 0; bb < 4; ++bb) { acc[bb][2 * k] = dot2(lo, xp[bb], acc[bb][2 * k]); acc[bb][2 * k + 1] = dot2(hi, xp[bb], acc[bb][2 * k + 1]); }
          }
          KEEP8(acc[0]); KEEP8(acc[1]); KEEP8(acc[2]); KEEP8(acc[3]);
          SB();
          __builtin_amdgcn_s_setprio(3);
          const int o = I0 == 0 ? mso + (8 + i) * istep : 0x40000000;
          m0[i] = __builtin_amdgcn_raw_buffer_load_b128(rm, mvo, o, AUX_M); SB();
          m1[i] = __builtin_amdgcn_raw_buffer_load_b128(rm, mvo, o + rowb, AUX_M); SB();
        }
      }
      if (!dn) {
#pragma unroll
        for (int bb = 0; bb < 4; ++bb) {
          *(f32x4*)&lds[L_YP + lane * 32 + bb * 8] = f32x4{acc[bb][0], acc[bb][1], acc[bb][2], acc[bb][3]};
          *(f32x4*)&lds[L_YP + lane * 32 + bb * 8 + 4] = f32x4{acc[bb][4], acc[bb][5], acc[bb][6], acc[bb][7]};
        }
        asm volatile("s_waitcnt lgkmcnt(0)" ::: "memory");
        f32x4 g4 = {0.f, 0.f, 0.f, 0.f};
        const float* sp = &lds[L_YP + (c >> 1) * 32 + r * 8 + (c & 1) * 4];
#pragma unroll
        for (int rg = 0; rg < 8; ++rg) g4 += *(const f32x4*)(sp + rg * 256);
        __builtin_amdgcn_raw_buffer_store_b128(v4u{canon(g4[0]), canon(g4[1]), canon(g4[2]), canon(g4[3])}, rw, lane * 16, WS_GU + rho * 1024, AUX_SC1);
      } else {
#pragma unroll
        for (int bb = 0; bb < 4; ++bb) {
          const float f = 1.0f - gate[bb];
          const v4u y0 = {canon(acc[bb][0] * f), canon(acc[bb][1] * f), canon(acc[bb][2] * f), canon(acc[bb][3] * f)};
          const v4u y1 = {canon(acc[bb][4] * f), canon(acc[bb][5] * f), canon(acc[bb][6] * f), canon(acc[bb][7] * f)};
          __builtin_amdgcn_raw_buffer_store_b128(y0, rw, lane * 32, WS_YM + sl * 8192 + bb * 2048, AUX_SC1);
          __builtin_amdgcn_raw_buffer_store_b128(y1, rw, lane * 32 + 16, WS_YM + sl * 8192 + bb * 2048, AUX_SC1);
        }
      }
    }
    STAMP(1);
    lds_wait(&lds[L_SC + 2], NW);
    const float mw = lds[L_M + (lane & 7)];
    float M = fmaxf(mw, dpp<XOR1>(mw)); M = fmaxf(M, dpp<XOR2>(M)); M = fmaxf(M, dpp<XOR7>(M));
    const float ew = __builtin_amdgcn_exp2f(mw - M);
    float L = ew * lds[L_L + (lane & 7)]; L += dpp<XOR1>(L); L += dpp<XOR2>(L); L += dpp<XOR7>(L);
    if (lane == 0) __hip_atomic_store(ML + chunk, ((unsigned long long)canon(L) << 32) | canon(M), RLX_AGENT);
    if (!dn) WAIT_GO();
    lds_wait(&lds[L_SC + 3], 6);
    unsigned long long pm = ~0ull;
#pragma unroll 1
    for (unsigned spin = 0; spin < SPIN_CAP; ++spin) {
      POLL_FENCE();
      pm = __hip_atomic_load(ML + (chunk ^ 1), RLX_AGENT);
      if (__builtin_amdgcn_readfirstlane((unsigned)pm) != SENT) break;
      __builtin_amdgcn_s_sleep(32);
    }
    {
      const float M2 = __uint_as_float((unsigned)pm), L2 = __uint_as_float((unsigned)(pm >> 32));
      const float Mx = fmaxf(M, M2), e1 = __builtin_amdgcn_exp2f(M - Mx), e2 = __builtin_amdgcn_exp2f(M2 - Mx);
      if (lane < 8) lds[L_F + lane] = ew * gate_b * e1 / (e1 * L + e2 * L2);
    }
    STAMP(2);
    const int u = chunk & 63;
    float xs[8];
#pragma unroll
    for (int k = 0; k < 8; ++k) xs[k] = 0.f;
#pragma unroll 1
    for (int round = 0; round < 2; ++round) {
      const int yo = round ? WS_Y + (b * 64) * 2048 + u * 32 : WS_YM + b * 2048 + u * 32;
      const int vo = round ? lane * 2048 : lane * 8192;
      v4u y0 = {0u, 0u, 0u, 0u}, y1 = y0;
      bool ok = !round && (NOMLP || lane >= NSL);
      BAR();
      if (round) { STAMP(3); }
#pragma unroll 1
      for (unsigned spin = 0; spin < SPIN_CAP; ++spin) {
        POLL_FENCE();
        if (!ok) {
          y0 = __builtin_amdgcn_raw_buffer_load_b128(rw, vo, yo, AUX_SC1);
          y1 = __builtin_amdgcn_raw_buffer_load_b128(rw, vo, yo + 16, AUX_SC1);
          ok = y0[0] != SENT && y0[1] != SENT && y0[2] != SENT && y0[3] != SENT && y1[0] != SENT && y1[1] != SENT && y1[2] != SENT && y1[3] != SENT;
        }
        if (__all(ok)) break;
        __builtin_amdgcn_s_sleep(2);
      }
#pragma unroll
      for (int k = 0; k < 4; ++k) { xs[k] += __uint_as_float(y0[k]); xs[4 + k] += __uint_as_float(y1[k]); }
    }
#define TR_STEP(HALF, CTRL, BIT) _Pragma("unroll") for (int hh = 0; hh < (HALF); ++hh) { \
      const float lo_ = xs[hh], hi_ = xs[hh + (HALF)]; const float t1_ = lo_ + dpp<CTRL>(lo_), t2_ = hi_ + dpp<CTRL>(hi_); \
      xs[hh] = (c & (BIT)) ? t2_ : t1_; }
    TR_STEP(4, XOR8, 8) TR_STEP(2, XOR7, 4) TR_STEP(1, XOR2, 2)
    float o = xs[0]; o += dpp<XOR1>(o); o = xrow_sum(o);
    if (lane < 16 && !(lane & 1)) out[b * DM + u * 8 + (lane >> 1)] = __builtin_bit_cast(unsigned short, (__bf16)o);
    STAMP(7);
    return;
  }

  const __amdgpu_buffer_rsrc_t rk = __builtin_amdgcn_make_buffer_rsrc((void*)kc, 0, NB * HQ * SK * DH * 2, RSW3);
  const __amdgpu_buffer_rsrc_t rv = __builtin_amdgcn_make_buffer_rsrc((void*)vc, 0, NB * HQ * SK * DH * 2, RSW3);
  const __amdgpu_buffer_rsrc_t rq = __builtin_amdgcn_make_buffer_rsrc((void*)wq, 0, ZQ ? 0 : DM * HQ * DH * 2, RSW3);
  const __amdgpu_buffer_rsrc_t ro = __builtin_amdgcn_make_buffer_rsrc((void*)wo, 0, ZO ? 0 : DM * HQ * DH * 2, RSW3);

  const int kvoff = lane * 16;
  const int kvbase = chunk * (512 * 1024) + w * 4096;
  const int r0 = ROTM == 0 ? chunk * 4 : ROTM == 14 ? chunk * 4 + 4 * (chunk >> 3) : ROTM == 12 ? chunk * 4 + 4 * (chunk >> 4) : chunk * 4 + 4 * (chunk >> 6);
#define KVOFF(t) (kvbase + ((((t) + r0) & 15) << 15))
  v4u wq_[16], wt[16];
  v4u a[4 * RG];
  unsigned xd[8];
  const int oso = (h * DH + 16 * w) * (DM * 2);
  const int qvoff = r * 2 * (HQ * DH * 2) + c * 16, qso = 8 * w * (HQ * DH * 2) + h * DH * 2;
  const int rot = 2 * b;
#define KLOAD(n) { a[n] = __builtin_amdgcn_raw_buffer_load_b128(rk, kvoff, KVOFF((n) >> 2) + ((n) & 3) * 1024, AUX_K); SB(); }
#define LD_O(i) { wt[i] = __builtin_amdgcn_raw_buffer_load_b128(ro, kvoff, oso + (((i) + 2 * m8) & 15) * (DM * 2), AUX_W); SB(); }
#define LD_Q(i) { wq_[i] = __builtin_amdgcn_raw_buffer_load_b128(rq, qvoff, qso + (64 * ((((i) >> 1) + rot) & 7) + ((i) & 1)) * (HQ * DH * 2), AUX_WQ); SB(); }
#define LD_X() { _Pragma("unroll") for (int k = 0; k < 8; ++k) xd[k] = __builtin_amdgcn_raw_buffer_load_b32(rx, r * 4, (b * DM + 64 * ((k + rot) & 7) + 8 * w) * 2, 0); SB(); }
#define QPROJ() { \
    float qa[8]; \
    _Pragma("unroll") for (int k = 0; k < 8; ++k) qa[k] = 0.f; \
    _Pragma("unroll") for (int i = 0; i < 16; ++i) { \
      const unsigned xx = xd[i >> 1]; \
      const unsigned xl = (i & 1) ? (xx >> 16) : (xx & 0xffffu), xh = (i & 1) ? (xx & 0xffff0000u) : (xx << 16); \
      _Pragma("unroll") for (int k = 0; k < 4; ++k) { qa[2 * k] = dot2(wq_[i][k], xl, qa[2 * k]); qa[2 * k + 1] = dot2(wq_[i][k], xh, qa[2 * k + 1]); } \
    } \
    _Pragma("unroll") for (int k = 0; k < 8; ++k) qa[k] = xrow_sum(qa[k]); \
    KEEP8(qa); \
    if (r == 0) { \
      *(f32x4*)&lds[L_QP + w * 128 + c * 8] = f32x4{qa[0], qa[1], qa[2], qa[3]}; \
      *(f32x4*)&lds[L_QP + w * 128 + c * 8 + 4] = f32x4{qa[4], qa[5], qa[6], qa[7]}; \
    } \
    SB(); }
  const bool early = s == 0;
  if (early) {
#pragma unroll
    for (int i = 0; i < 12; ++i) LD_Q(i)
    LD_X()
#pragma unroll
    for (int n = 0; n < K0; ++n) KLOAD(n)
#pragma unroll
    for (int i = 12; i < 16; ++i) LD_Q(i)
  } else {
#pragma unroll
    for (int n = 0; n < 4 * RG; ++n) KLOAD(n)
  }
  BAR();
  SB();
  STAMP(1);
  if (!early) {
    LD_X()
#pragma unroll
    for (int i = 0; i < 16; ++i) LD_Q(i)
  }
  QPROJ()
  if (early) {
#pragma unroll
    for (int n = K0; n < 4 * RG; ++n) KLOAD(n)
  }
  BAR();
  STAMP(2);
  unsigned qpk[4];
  {
    f32x4 q0 = {0.f, 0.f, 0.f, 0.f}, q1 = q0;
#pragma unroll
    for (int ww = 0; ww < NW; ++ww) { q0 += *(const f32x4*)&lds[L_QP + ww * 128 + c * 8]; q1 += *(const f32x4*)&lds[L_QP + ww * 128 + c * 8 + 4]; }
    qpk[0] = pk_bf16(q0[0], q0[1]); qpk[1] = pk_bf16(q0[2], q0[3]); qpk[2] = pk_bf16(q1[0], q1[1]); qpk[3] = pk_bf16(q1[2], q1[3]);
    asm volatile("" : "+v"(qpk[0]), "+v"(qpk[1]), "+v"(qpk[2]), "+v"(qpk[3]));
    SB();
  }
  STAMP(3);
  float* prow = &lds[L_P + (w * 4 + r) * 64];
  float mx = -INFINITY;
#define PACE_IN(st) { if (SLACK && (st) >= SLACK) lds_wait(&lds[L_SC + 1], NW * ((st) - SLACK + 1)); }
#define PACE_OUT() { if (SLACK && lane == 0) __hip_atomic_fetch_add((lu32*)&lds[L_SC + 1], 1u, __ATOMIC_RELAXED, __HIP_MEMORY_SCOPE_WORKGROUP); }
#define KSTEP(T, RN, AUXN) { \
    PACE_IN(T) \
    float xs[16]; \
    _Pragma("unroll") for (int g = 0; g < 4; ++g) { \
      const int o = KVOFF(4 * (T) + g + RG); \
      _Pragma("unroll") for (int i = 0; i < 4; ++i) { \
        v4u& kk = a[(g % RG) * 4 + i]; \
        float d = dot2(kk[0], qpk[0], 0.f); d = dot2(kk[1], qpk[1], d); d = dot2(kk[2], qpk[2], d); d = dot2(kk[3], qpk[3], d); \
        asm volatile("" : "+v"(d)); \
        xs[g * 4 + i] = d; \
        SB(); \
        kk = __builtin_amdgcn_raw_buffer_load_b128(RN, kvoff, o + i * 1024, AUXN); \
        SB(); \
      } \
      LD_O(4 * (T) + g) \
    } \
    PACE_OUT() \
    TR_STEP(8, XOR8, 8) TR_STEP(4, XOR7, 4) TR_STEP(2, XOR2, 2) TR_STEP(1, XOR1, 1) \
    const float sc = xs[0] * (SCALE * LOG2E); \
    prow[16 * (T) + c] = sc; \
    mx = fmaxf(mx, sc); \
    if (tid == 64 * (NW - 1)) __hip_atomic_store((lu32*)&lds[L_SC + 3], (unsigned)(T) + 1u, __ATOMIC_RELAXED, __HIP_MEMORY_SCOPE_WORKGROUP); }
  static_assert(RG == 4, "the last K step refills the whole ring from V");
  KSTEP(0, rk, AUX_K) KSTEP(1, rk, AUX_K) KSTEP(2, rk, AUX_K) KSTEP(3, rv, AUX_V)
  STAMP(4);
  mx = xrow_max(row_max(mx));
  {
    float ls = 0.f;
#pragma unroll
    for (int T = 0; T < 4; ++T) { const float p = __builtin_amdgcn_exp2f(prow[16 * T + c] - mx); prow[16 * T + c] = p; ls += p; }
    ls = xrow_sum(row_sum(ls));
    if (lane == 0) { lds[L_M + w] = mx; lds[L_L + w] = ls; lds_arrive(&lds[L_SC + 2]); }
  }
  asm volatile("s_waitcnt lgkmcnt(0)" ::: "memory");
  SB();
  STAMP(5);
#define WPERM(p) { _Pragma("unroll") for (int k = 0; k < 4; ++k) { const unsigned lo_ = __builtin_amdgcn_perm(wt[2 * (p) + 1][k], wt[2 * (p)][k], 0x05040100u), \
      hi_ = __builtin_amdgcn_perm(wt[2 * (p) + 1][k], wt[2 * (p)][k], 0x07060302u); wt[2 * (p)][k] = lo_; wt[2 * (p) + 1][k] = hi_; } \
      asm volatile("" : "+v"(wt[2 * (p)]), "+v"(wt[2 * (p) + 1])); }
  WPERM(0) WPERM(1) WPERM(2) WPERM(3) WPERM(4) WPERM(5)
  SB();
  float oa[8];
#pragma unroll
  for (int k = 0; k < 8; ++k) oa[k] = 0.f;
#pragma unroll 1
  for (int T = 0; T < 4; ++T) {
    if (!SLACK && T == PACE) lds_wait(&lds[L_SC + 2], NW);
    PACE_IN(4 + T)
#pragma unroll
    for (int g = 0; g < 4; ++g) {
      const int un = 4 * T + g + RG;
      const int o = un < 16 ? KVOFF(un) : 0x40000000;
      const f32x4 p4 = *(const f32x4*)&prow[16 * T + 4 * g];
#pragma unroll
      for (int i = 0; i < 4; ++i) {
        v4u& vv = a[(g % RG) * 4 + i];
#pragma unroll
        for (int k = 0; k < 4; ++k) { oa[2 * k] = fmaf(p4[i], bflo(vv[k]), oa[2 * k]); oa[2 * k + 1] = fmaf(p4[i], bfhi(vv[k]), oa[2 * k + 1]); }
        KEEP8(oa);
        SB();
        vv = __builtin_amdgcn_raw_buffer_load_b128(rv, kvoff, o + i * 1024, AUX_V);
        SB();
      }
    }
    PACE_OUT()
    if (tid == 64 * (NW - 1)) __hip_atomic_store((lu32*)&lds[L_SC + 3], (unsigned)T + 5u, __ATOMIC_RELAXED, __HIP_MEMORY_SCOPE_WORKGROUP);
  }
  WPERM(6) WPERM(7)
#pragma unroll
  for (int k = 0; k < 8; ++k) oa[k] = xrow_sum(oa[k]);
  STAMP(6);
  if (r == 0) {
    *(f32x4*)&lds[L_OP + w * 128 + c * 8] = f32x4{oa[0], oa[1], oa[2], oa[3]};
    *(f32x4*)&lds[L_OP + w * 128 + c * 8 + 4] = f32x4{oa[4], oa[5], oa[6], oa[7]};
  }
  BAR();
  float od = 0.f;
#pragma unroll
  for (int ww = 0; ww < NW; ++ww) od = fmaf(lds[L_F + ww], lds[L_OP + ww * 128 + 16 * w + ((c + 2 * m8) & 15)], od);
  const unsigned opk = pk_bf16(od, dpp<XOR1>(od));
  float ya[8];
#pragma unroll
  for (int k = 0; k < 8; ++k) ya[k] = 0.f;
#pragma unroll
  for (int p = 0; p < 8; ++p) {
    const unsigned ov = __builtin_amdgcn_readlane(opk, 2 * p);
#pragma unroll
    for (int k = 0; k < 4; ++k) { ya[2 * k] = dot2(wt[2 * p][k], ov, ya[2 * k]); ya[2 * k + 1] = dot2(wt[2 * p + 1][k], ov, ya[2 * k + 1]); }
  }
  *(f32x4*)&lds[L_YP + w * 512 + lane * 8] = f32x4{ya[0], ya[1], ya[2], ya[3]};
  *(f32x4*)&lds[L_YP + w * 512 + lane * 8 + 4] = f32x4{ya[4], ya[5], ya[6], ya[7]};
  BAR();
  if (w < 2) {
    f32x4 y = *(const f32x4*)&lds[L_YP + tid * 4];
#pragma unroll
    for (int ww = 1; ww < NW; ++ww) y += *(const f32x4*)&lds[L_YP + ww * 512 + tid * 4];
    const v4u yu = {canon(y[0]), canon(y[1]), canon(y[2]), canon(y[3])};
    __builtin_amdgcn_raw_buffer_store_b128(yu, rw, tid * 16, WS_Y + chunk * 2048, AUX_SC1);
  }
}
}

extern "C" void kernel_launch(void* const* d_in, const int* in_sizes, int n_in, void* d_out, int out_size, void* d_ws, size_t ws_size,
                              hipStream_t stream) {
  typedef const unsigned short* P;
  hipLaunchKernelGGL(k_main, dim3(NWG), dim3(NTHR), 0, stream, (P)d_in[1], (P)d_in[3], (P)d_in[0], (P)d_in[2], (P)d_in[4], (unsigned char*)d_ws,
                     (P)d_in[9], (unsigned short*)d_out, (P)d_in[6], (P)d_in[7], (P)d_in[8]);
}
```

```cpp
#include <hip/hip_runtime.h>
#include <hip/hip_bf16.h>
#include <cstdint>

namespace {
typedef unsigned v4u __attribute__((ext_vector_type(4)));
typedef unsigned v2u __attribute__((ext_vector_type(2)));
typedef float f32x4 __attribute__((ext_vector_type(4)));
typedef __bf16 bf16x2 __attribute__((ext_vector_type(2)));
#define GAS __attribute__((address_space(1)))
typedef GAS unsigned gu32;
typedef GAS unsigned long long gu64;
typedef __attribute__((address_space(3))) unsigned lu32;
#define RLX_AGENT __ATOMIC_RELAXED, __HIP_MEMORY_SCOPE_AGENT
#define RLX_WG __ATOMIC_RELAXED, __HIP_MEMORY_SCOPE_WORKGROUP

constexpr int NB = 4, DM = 512, HQ = 32, SK = 4096, DH = 128, FF = 1376;
constexpr int NWG = 256, NW = 8, NTHR = 64 * (NW + 1);
constexpr int NSL = 43;
constexpr int NGU = 88;
constexpr int RSW3 = 0x00020000;
constexpr int AUX_NT = 2, AUX_SC1 = 16;
constexpr int OOB = 0x40000000;
constexpr float LOG2E = 1.4426950408889634f;
constexpr float SCALE = 0.08838834764831845f;
constexpr unsigned SENT = 0xFFFFFFFFu;
constexpr unsigned SPIN_CAP = 1u << 18;
constexpr int K0 = 4;

constexpr int LS = 384;
constexpr int WS_R = 0;
constexpr int WS_E = 64 * LS;
constexpr int WS_ML = 128 * LS;
constexpr int WS_Y = (WS_ML + 128 * LS + 4095) & ~4095;
constexpr int WS_YM = WS_Y + NWG * 2048;
constexpr int WS_GU = WS_YM + NSL * 8192;
constexpr int WS_END = WS_GU + NGU * 1024;

constexpr int L_QP = 0;
constexpr int L_P = L_QP + 8 * 128;
constexpr int L_M = L_P + 8 * 256;
constexpr int L_L = L_M + 8;
constexpr int L_SC = L_L + 8;
constexpr int L_F = L_SC + 8;
constexpr int L_OP = L_F + 8;
constexpr int L_YP = L_OP + 8 * 128;
constexpr int L_XT = L_YP + 8 * 512;
constexpr int L_END = L_XT + 272 * 4;

__device__ __forceinline__ float dot2(unsigned a, unsigned b, float acc) {
  return __builtin_amdgcn_fdot2_f32_bf16(__builtin_bit_cast(bf16x2, a), __builtin_bit_cast(bf16x2, b), acc, false);
}
__device__ __forceinline__ float bflo(unsigned u) { return __builtin_bit_cast(float, u << 16); }
__device__ __forceinline__ float bfhi(unsigned u) { return __builtin_bit_cast(float, u & 0xffff0000u); }
__device__ __forceinline__ unsigned pk_bf16(float lo, float hi) {
  bf16x2 r; r.x = (__bf16)lo; r.y = (__bf16)hi; return __builtin_bit_cast(unsigned, r);
}
template <int CTRL> __device__ __forceinline__ float dpp(float x) {
  return __builtin_bit_cast(float, __builtin_amdgcn_mov_dpp(__builtin_bit_cast(int, x), CTRL, 0xf, 0xf, true));
}
constexpr int XOR1 = 0xB1, XOR2 = 0x4E, XOR7 = 0x141, XOR8 = 0x128;
__device__ __forceinline__ float xrow_sum(float x) {
  auto s = __builtin_amdgcn_permlane16_swap(__float_as_uint(x), __float_as_uint(x), false, false);
  x = __uint_as_float(s[0]) + __uint_as_float(s[1]);
  auto t = __builtin_amdgcn_permlane32_swap(__float_as_uint(x), __float_as_uint(x), false, false);
  return __uint_as_float(t[0]) + __uint_as_float(t[1]);
}
__device__ __forceinline__ float xrow_max(float x) {
  auto s = __builtin_amdgcn_permlane16_swap(__float_as_uint(x), __float_as_uint(x), false, false);
  x = fmaxf(__uint_as_float(s[0]), __uint_as_float(s[1]));
  auto t = __builtin_amdgcn_permlane32_swap(__float_as_uint(x), __float_as_uint(x), false, false);
  return fmaxf(__uint_as_float(t[0]), __uint_as_float(t[1]));
}
__device__ __forceinline__ float row_sum(float x) { x += dpp<XOR1>(x); x += dpp<XOR2>(x); x += dpp<XOR7>(x); x += dpp<XOR8>(x); return x; }
__device__ __forceinline__ float row_max(float x) { x = fmaxf(x, dpp<XOR1>(x)); x = fmaxf(x, dpp<XOR2>(x)); x = fmaxf(x, dpp<XOR7>(x)); x = fmaxf(x, dpp<XOR8>(x)); return x; }
__device__ __forceinline__ float sigmoidf_(float x) { return 1.0f / (1.0f + __expf(-x)); }
__device__ __forceinline__ unsigned canon(float v) { const unsigned u = __float_as_uint(v); return u == SENT ? 0x7FC00000u : u; }
#define SB() __builtin_amdgcn_sched_barrier(0)
#define POLL_FENCE() asm volatile("" ::: "memory")
#define DRAIN() asm volatile("s_waitcnt vmcnt(0)" ::: "memory")
#define LDS_DONE() asm volatile("s_waitcnt lgkmcnt(0)" ::: "memory")
#define BAR() do { LDS_DONE(); __builtin_amdgcn_s_barrier(); asm volatile("" ::: "memory"); } while (0)
#define KEEP8(a) asm volatile("" : "+v"(a[0]), "+v"(a[1]), "+v"(a[2]), "+v"(a[3]), "+v"(a[4]), "+v"(a[5]), "+v"(a[6]), "+v"(a[7]))
#define TR_STEP(HALF, CTRL, BIT) _Pragma("unroll") for (int hh = 0; hh < (HALF); ++hh) { \
      const float lo_ = xs[hh], hi_ = xs[hh + (HALF)]; const float t1_ = lo_ + dpp<CTRL>(lo_), t2_ = hi_ + dpp<CTRL>(hi_); \
      xs[hh] = (c & (BIT)) ? t2_ : t1_; }

__device__ __forceinline__ void lds_arrive(float* p) { LDS_DONE(); __hip_atomic_fetch_add((lu32*)p, 1u, RLX_WG); }
__device__ __forceinline__ void lds_wait(float* p, unsigned n) {
#pragma unroll 1
  for (unsigned spin = 0; spin < (SPIN_CAP << 4); ++spin) {
    if (__builtin_amdgcn_readfirstlane(__hip_atomic_load((lu32*)p, RLX_WG)) >= n) break;
    __builtin_amdgcn_s_sleep(1);
  }
  asm volatile("" ::: "memory");
}

__global__ void __launch_bounds__(NTHR, 3)
k_main(const unsigned short* __restrict__ kc, const unsigned short* __restrict__ wq, const unsigned short* __restrict__ x,
       const unsigned short* __restrict__ vc, const unsigned short* __restrict__ wo, unsigned char* __restrict__ ws,
       const unsigned short* __restrict__ gw, unsigned short* __restrict__ out,
       const unsigned short* __restrict__ wg, const unsigned short* __restrict__ wu, const unsigned short* __restrict__ wd) {
  __shared__ __attribute__((aligned(16))) float lds[L_END];
  asm volatile("" :: "s"(x), "s"(kc), "s"(vc), "s"(wq), "s"(wo), "s"(ws), "s"(gw), "s"(out), "s"(wg), "s"(wu), "s"(wd));
  const int tid = threadIdx.x, lane = tid & 63, w = __builtin_amdgcn_readfirstlane(tid >> 6), r = lane >> 4, c = lane & 15;
  const int bid = blockIdx.x, xg = bid & 7, j = bid >> 3;
  const int h = xg * 4 + (j >> 3), b = (j >> 1) & 3, s = j & 1, m8 = j & 7;
  const int chunk = (b * HQ + h) * 2 + s;
  const __amdgpu_buffer_rsrc_t rx = __builtin_amdgcn_make_buffer_rsrc((void*)x, 0, NB * DM * 2, RSW3);
  const __amdgpu_buffer_rsrc_t rw = __builtin_amdgcn_make_buffer_rsrc((void*)ws, 0, WS_END, RSW3);

  if (w == NW) {
    gu32* R = (gu32*)(ws + WS_R + (chunk >> 2) * LS) + (chunk & 3);
    gu32* E = (gu32*)(ws + WS_E + (chunk >> 2) * LS) + (chunk & 3);
    gu64* ML = (gu64*)(ws + WS_ML + (chunk >> 1) * LS);
    __builtin_amdgcn_s_setprio(3);
    if (lane < 3) ((unsigned*)lds)[L_SC + 1 + lane] = 0u;
    const int rho = (bid & 1) ? (bid < 8 ? 127 + (bid >> 1) : 999) : (bid >> 1) - 1;
    const bool gu = (unsigned)rho < (unsigned)NGU, dn = rho >= NGU && rho < NGU + NSL;
    const int sl = rho - NGU;
    const unsigned r_old = __hip_atomic_load(R, RLX_AGENT), e_old = __hip_atomic_load(E, RLX_AGENT);
    const v4u rfirst = __builtin_amdgcn_raw_buffer_load_b128(rw, lane * LS, bid == 0 ? WS_R : OOB, AUX_SC1);
    v4u xv[4];
#pragma unroll
    for (int bb = 0; bb < 4; ++bb) xv[bb] = __builtin_amdgcn_raw_buffer_load_b128(rx, lane * 16, bb * DM * 2, 0);
    const v4u gv = *(const GAS v4u*)((const GAS unsigned char*)gw + lane * 16);
    BAR();
    BAR();
    const v4u sent4 = {SENT, SENT, SENT, SENT};
    __builtin_amdgcn_raw_buffer_store_b128(sent4, rw, lane * 16, WS_Y + chunk * 2048, AUX_SC1);
    __builtin_amdgcn_raw_buffer_store_b128(sent4, rw, lane * 16, WS_Y + chunk * 2048 + 1024, AUX_SC1);
    if (dn) {
#pragma unroll 1
      for (int i = 0; i < 8; ++i) __builtin_amdgcn_raw_buffer_store_b128(sent4, rw, lane * 16, WS_YM + sl * 8192 + i * 1024, AUX_SC1);
    }
    if (gu) __builtin_amdgcn_raw_buffer_store_b128(sent4, rw, lane * 16, WS_GU + rho * 1024, AUX_SC1);
    if (lane == 0) __hip_atomic_store(ML + s, ~0ull, RLX_AGENT);
    unsigned nonce = (r_old & ~3u) + 4u; if (e_old - nonce < 4u) nonce += 4u;
    float gate[4];
#pragma unroll
    for (int bb = 0; bb < 4; ++bb) {
      float gd = dot2(xv[bb][0], gv[0], 0.f); gd = dot2(xv[bb][1], gv[1], gd); gd = dot2(xv[bb][2], gv[2], gd); gd = dot2(xv[bb][3], gv[3], gd);
      gate[bb] = sigmoidf_(xrow_sum(row_sum(gd)));
    }
    const float gate_b = b == 0 ? gate[0] : b == 1 ? gate[1] : b == 2 ? gate[2] : gate[3];
#pragma unroll
    for (int k = 0; k < 4; ++k) *(v4u*)&lds[L_XT + (4 * lane + k) * 4] = v4u{xv[0][k], xv[1][k], xv[2][k], xv[3][k]};
    DRAIN();
    if (lane == 0) __hip_atomic_store(R, nonce, RLX_AGENT);
    const __amdgpu_buffer_rsrc_t rm = __builtin_amdgcn_make_buffer_rsrc((void*)(dn ? wd : (rho & 2) ? wu : wg), 0, (gu || dn) ? DM * FF * 2 : 0, RSW3);
    const int rowb = dn ? DM * 2 : FF * 2;
    const int mvo = dn ? lane * 16 : (lane >> 3) * 2 * (FF * 2) + (lane & 7) * 16;
    const int mso = dn ? sl * 32 * (DM * 2) : (rho & 1) * 256 * (FF * 2) + (rho >> 2) * 128, istep = dn ? 2 * (DM * 2) : 16 * (FF * 2);
    v4u m0[8], m1[8];
    if (gu || dn) {
#pragma unroll
      for (int i = 0; i < 8; ++i) {
        m0[i] = __builtin_amdgcn_raw_buffer_load_b128(rm, mvo, mso + i * istep, AUX_NT); SB();
        m1[i] = __builtin_amdgcn_raw_buffer_load_b128(rm, mvo, mso + i * istep + rowb, AUX_NT); SB();
      }
    }
    if (bid == 0) {
      unsigned seen = 0;
#pragma unroll 1
      for (unsigned spin = 0; spin < SPIN_CAP; ++spin) {
        POLL_FENCE();
        __builtin_amdgcn_s_sleep(40);
        const v4u rq = __builtin_amdgcn_raw_buffer_load_b128(rw, lane * LS, WS_R, AUX_SC1);
#pragma unroll
        for (int k = 0; k < 4; ++k) seen |= (rq[k] != rfirst[k]) ? 1u << k : 0u;
        if (__all(seen == 15u)) {
          __builtin_amdgcn_raw_buffer_store_b128((rq & ~3u) + 2u, rw, lane * LS, WS_E, AUX_SC1);
          break;
        }
        if (spin >= 2) {
#pragma unroll
          for (int k = 0; k < 4; ++k) if (!(seen >> k & 1u)) __hip_atomic_store((gu32*)(ws + WS_E + lane * LS) + k, rq[k], RLX_AGENT);
        }
      }
    }
#define WAIT_GO() do { _Pragma("unroll 1") for (unsigned spin = 0; spin < SPIN_CAP; ++spin) { POLL_FENCE(); \
        const unsigned e_ = __builtin_amdgcn_readfirstlane(__hip_atomic_load(E, RLX_AGENT)); \
        if (e_ == nonce + 2u) break; \
        if (e_ == nonce && lane == 0) __hip_atomic_store(R, nonce + 1u, RLX_AGENT); \
        __builtin_amdgcn_s_sleep(32); } } while (0)
    if (gu || dn) {
      if (dn) {
        lds_wait(&lds[L_SC + 3], 3);
        WAIT_GO();
        v2u pv[4];
        const int go = WS_GU + (sl >> 1) * 4096 + (sl & 1) * 128, gvo = r * 256 + c * 8;
#pragma unroll 1
        for (unsigned spin = 0; spin < SPIN_CAP; ++spin) {
          POLL_FENCE();
          bool ok = true;
#pragma unroll
          for (int k = 0; k < 4; ++k) { pv[k] = __builtin_amdgcn_raw_buffer_load_b64(rw, gvo, go + k * 1024, AUX_SC1); }
#pragma unroll
          for (int k = 0; k < 4; ++k) ok = ok && pv[k][0] != SENT && pv[k][1] != SENT;
          if (__all(ok)) break;
          __builtin_amdgcn_s_sleep(32);
        }
        const float g0 = __uint_as_float(pv[0][0]) + __uint_as_float(pv[1][0]), g1 = __uint_as_float(pv[0][1]) + __uint_as_float(pv[1][1]);
        const float u0 = __uint_as_float(pv[2][0]) + __uint_as_float(pv[3][0]), u1 = __uint_as_float(pv[2][1]) + __uint_as_float(pv[3][1]);
        ((unsigned*)lds)[L_XT + (256 + c) * 4 + r] = pk_bf16(sigmoidf_(g0) * u0, sigmoidf_(g1) * u1);
        LDS_DONE();
      }
      const float* xt = &lds[L_XT + (dn ? 256 * 4 : ((rho & 1) * 128 + (lane >> 3)) * 4)];
      const int xstep = dn ? 4 : 32;
      float acc[4][8];
#pragma unroll
      for (int bb = 0; bb < 4; ++bb)
#pragma unroll
        for (int k = 0; k < 8; ++k) acc[bb][k] = 0.f;
#pragma unroll 1
      for (int I0 = 0; I0 < 16; I0 += 8) {
#pragma unroll
        for (int i = 0; i < 8; ++i) {
          __builtin_amdgcn_s_setprio(0);
          const v4u xp = *(const v4u*)(xt + (I0 + i) * xstep);
#pragma unroll
          for (int k = 0; k < 4; ++k) {
            const unsigned lo = __builtin_amdgcn_perm(m1[i][k], m0[i][k], 0x05040100u), hi = __builtin_amdgcn_perm(m1[i][k], m0[i][k], 0x07060302u);
#pragma unroll
            for (int bb = 0; bb < 4; ++bb) { acc[bb][2 * k] = dot2(lo, xp[bb], acc[bb][2 * k]); acc[bb][2 * k + 1] = dot2(hi, xp[bb], acc[bb][2 * k + 1]); }
          }
          KEEP8(acc[0]); KEEP8(acc[1]); KEEP8(acc[2]); KEEP8(acc[3]);
          SB();
          __builtin_amdgcn_s_setprio(3);
          const int o = I0 == 0 ? mso + (8 + i) * istep : OOB;
          m0[i] = __builtin_amdgcn_raw_buffer_load_b128(rm, mvo, o, AUX_NT); SB();
          m1[i] = __builtin_amdgcn_raw_buffer_load_b128(rm, mvo, o + rowb, AUX_NT); SB();
        }
      }
      if (!dn) {
#pragma unroll
        for (int bb = 0; bb < 4; ++bb) {
          *(f32x4*)&lds[L_YP + lane * 32 + bb * 8] = f32x4{acc[bb][0], acc[bb][1], acc[bb][2], acc[bb][3]};
          *(f32x4*)&lds[L_YP + lane * 32 + bb * 8 + 4] = f32x4{acc[bb][4], acc[bb][5], acc[bb][6], acc[bb][7]};
        }
        LDS_DONE();
        f32x4 g4 = {0.f, 0.f, 0.f, 0.f};
        const float* sp = &lds[L_YP + (c >> 1) * 32 + r * 8 + (c & 1) * 4];
#pragma unroll
        for (int rg = 0; rg < 8; ++rg) g4 += *(const f32x4*)(sp + rg * 256);
        __builtin_amdgcn_raw_buffer_store_b128(v4u{canon(g4[0]), canon(g4[1]), canon(g4[2]), canon(g4[3])}, rw, lane * 16, WS_GU + rho * 1024, AUX_SC1);
      } else {
#pragma unroll
        for (int bb = 0; bb < 4; ++bb) {
          const float f = 1.0f - gate[bb];
          const v4u y0 = {canon(acc[bb][0] * f), canon(acc[bb][1] * f), canon(acc[bb][2] * f), canon(acc[bb][3] * f)};
          const v4u y1 = {canon(acc[bb][4] * f), canon(acc[bb][5] * f), canon(acc[bb][6] * f), canon(acc[bb][7] * f)};
          __builtin_amdgcn_raw_buffer_store_b128(y0, rw, lane * 32, WS_YM + sl * 8192 + bb * 2048, AUX_SC1);
          __builtin_amdgcn_raw_buffer_store_b128(y1, rw, lane * 32 + 16, WS_YM + sl * 8192 + bb * 2048, AUX_SC1);
        }
      }
    }
    lds_wait(&lds[L_SC + 2], NW);
    const float mw = lds[L_M + (lane & 7)];
    float M = fmaxf(mw, dpp<XOR1>(mw)); M = fmaxf(M, dpp<XOR2>(M)); M = fmaxf(M, dpp<XOR7>(M));
    const float ew = __builtin_amdgcn_exp2f(mw - M);
    float L = ew * lds[L_L + (lane & 7)]; L += dpp<XOR1>(L); L += dpp<XOR2>(L); L += dpp<XOR7>(L);
    if (lane == 0) __hip_atomic_store(ML + s, ((unsigned long long)canon(L) << 32) | canon(M), RLX_AGENT);
    if (!dn) WAIT_GO();
    lds_wait(&lds[L_SC + 3], 6);
    unsigned long long pm = ~0ull;
#pragma unroll 1
    for (unsigned spin = 0; spin < SPIN_CAP; ++spin) {
      POLL_FENCE();
      pm = __hip_atomic_load(ML + (s ^ 1), RLX_AGENT);
      if (__builtin_amdgcn_readfirstlane((unsigned)pm) != SENT) break;
      __builtin_amdgcn_s_sleep(32);
    }
    {
      const float M2 = __uint_as_float((unsigned)pm), L2 = __uint_as_float((unsigned)(pm >> 32));
      const float Mx = fmaxf(M, M2), e1 = __builtin_amdgcn_exp2f(M - Mx), e2 = __builtin_amdgcn_exp2f(M2 - Mx);
      if (lane < 8) lds[L_F + lane] = ew * gate_b * e1 / (e1 * L + e2 * L2);
    }
    const int u = chunk & 63;
    float xs[8];
#pragma unroll
    for (int k = 0; k < 8; ++k) xs[k] = 0.f;
#pragma unroll 1
    for (int round = 0; round < 2; ++round) {
      const int yo = round ? WS_Y + (b * 64) * 2048 + u * 32 : WS_YM + b * 2048 + u * 32;
      const int vo = round ? lane * 2048 : lane * 8192;
      v4u y0 = {0u, 0u, 0u, 0u}, y1 = y0;
      bool ok = !round && lane >= NSL;
      BAR();
#pragma unroll 1
      for (unsigned spin = 0; spin < SPIN_CAP; ++spin) {
        POLL_FENCE();
        if (!ok) {
          y0 = __builtin_amdgcn_raw_buffer_load_b128(rw, vo, yo, AUX_SC1);
          y1 = __builtin_amdgcn_raw_buffer_load_b128(rw, vo, yo + 16, AUX_SC1);
          ok = y0[0] != SENT && y0[1] != SENT && y0[2] != SENT && y0[3] != SENT && y1[0] != SENT && y1[1] != SENT && y1[2] != SENT && y1[3] != SENT;
        }
        if (__all(ok)) break;
        __builtin_amdgcn_s_sleep(2);
      }
#pragma unroll
      for (int k = 0; k < 4; ++k) { xs[k] += __uint_as_float(y0[k]); xs[4 + k] += __uint_as_float(y1[k]); }
    }
    TR_STEP(4, XOR8, 8) TR_STEP(2, XOR7, 4) TR_STEP(1, XOR2, 2)
    float o = xs[0]; o += dpp<XOR1>(o); o = xrow_sum(o);
    if (lane < 16 && !(lane & 1)) out[b * DM + u * 8 + (lane >> 1)] = __builtin_bit_cast(unsigned short, (__bf16)o);
    return;
  }

  const __amdgpu_buffer_rsrc_t rk = __builtin_amdgcn_make_buffer_rsrc((void*)kc, 0, NB * HQ * SK * DH * 2, RSW3);
  const __amdgpu_buffer_rsrc_t rv = __builtin_amdgcn_make_buffer_rsrc((void*)vc, 0, NB * HQ * SK * DH * 2, RSW3);
  const __amdgpu_buffer_rsrc_t rq = __builtin_amdgcn_make_buffer_rsrc((void*)wq, 0, DM * HQ * DH * 2, RSW3);
  const __amdgpu_buffer_rsrc_t ro = __builtin_amdgcn_make_buffer_rsrc((void*)wo, 0, DM * HQ * DH * 2, RSW3);

  const int kvoff = lane * 16;
  const int kvbase = chunk * (512 * 1024) + w * 4096;
  const int r0 = chunk * 4;
#define KVOFF(t) (kvbase + ((((t) + r0) & 15) << 15))
  v4u wq_[16], wt[16];
  v4u a[16];
  unsigned xd[8];
  const int oso = (h * DH + 16 * w) * (DM * 2);
  const int qvoff = r * 2 * (HQ * DH * 2) + c * 16, qso = 8 * w * (HQ * DH * 2) + h * DH * 2;
  const int rot = 2 * b;
#define KLOAD(n) { a[n] = __builtin_amdgcn_raw_buffer_load_b128(rk, kvoff, KVOFF((n) >> 2) + ((n) & 3) * 1024, AUX_NT); SB(); }
#define LD_O(i) { wt[i] = __builtin_amdgcn_raw_buffer_load_b128(ro, kvoff, oso + (((i) + 2 * m8) & 15) * (DM * 2), 0); SB(); }
#define LD_Q(i) { wq_[i] = __builtin_amdgcn_raw_buffer_load_b128(rq, qvoff, qso + (64 * ((((i) >> 1) + rot) & 7) + ((i) & 1)) * (HQ * DH * 2), 0); SB(); }
#define LD_X() { _Pragma("unroll") for (int k = 0; k < 8; ++k) xd[k] = __builtin_amdgcn_raw_buffer_load_b32(rx, r * 4, (b * DM + 64 * ((k + rot) & 7) + 8 * w) * 2, 0); SB(); }
  const bool early = s == 0;
  if (early) {
#pragma unroll
    for (int i = 0; i < 12; ++i) LD_Q(i)
    LD_X()
#pragma unroll
    for (int n = 0; n < K0; ++n) KLOAD(n)
#pragma unroll
    for (int i = 12; i < 16; ++i) LD_Q(i)
  } else {
#pragma unroll
    for (int n = 0; n < 16; ++n) KLOAD(n)
  }
  BAR();
  SB();
  if (!early) {
    LD_X()
#pragma unroll
    for (int i = 0; i < 16; ++i) LD_Q(i)
  }
  {
    float qa[8];
#pragma unroll
    for (int k = 0; k < 8; ++k) qa[k] = 0.f;
#pragma unroll
    for (int i = 0; i < 16; ++i) {
      const unsigned xx = xd[i >> 1];
      const unsigned xl = (i & 1) ? (xx >> 16) : (xx & 0xffffu), xh = (i & 1) ? (xx & 0xffff0000u) : (xx << 16);
#pragma unroll
      for (int k = 0; k < 4; ++k) { qa[2 * k] = dot2(wq_[i][k], xl, qa[2 * k]); qa[2 * k + 1] = dot2(wq_[i][k], xh, qa[2 * k + 1]); }
    }
#pragma unroll
    for (int k = 0; k < 8; ++k) qa[k] = xrow_sum(qa[k]);
    KEEP8(qa);
    if (r == 0) {
      *(f32x4*)&lds[L_QP + w * 128 + c * 8] = f32x4{qa[0], qa[1], qa[2], qa[3]};
      *(f32x4*)&lds[L_QP + w * 128 + c * 8 + 4] = f32x4{qa[4], qa[5], qa[6], qa[7]};
    }
    SB();
  }
  if (early) {
#pragma unroll
    for (int n = K0; n < 16; ++n) KLOAD(n)
  }
  BAR();
  unsigned qpk[4];
  {
    f32x4 q0 = {0.f, 0.f, 0.f, 0.f}, q1 = q0;
#pragma unroll
    for (int ww = 0; ww < NW; ++ww) { q0 += *(const f32x4*)&lds[L_QP + ww * 128 + c * 8]; q1 += *(const f32x4*)&lds[L_QP + ww * 128 + c * 8 + 4]; }
    qpk[0] = pk_bf16(q0[0], q0[1]); qpk[1] = pk_bf16(q0[2], q0[3]); qpk[2] = pk_bf16(q1[0], q1[1]); qpk[3] = pk_bf16(q1[2], q1[3]);
    asm volatile("" : "+v"(qpk[0]), "+v"(qpk[1]), "+v"(qpk[2]), "+v"(qpk[3]));
    SB();
  }
  float* prow = &lds[L_P + (w * 4 + r) * 64];
  float mx = -INFINITY;
#define PACE_IN(st) { if ((st) >= 1) lds_wait(&lds[L_SC + 1], NW * (st)); }
#define PACE_OUT() { if (lane == 0) __hip_atomic_fetch_add((lu32*)&lds[L_SC + 1], 1u, RLX_WG); }
#define KSTEP(T, RN) { \
    PACE_IN(T) \
    float xs[16]; \
    _Pragma("unroll") for (int g = 0; g < 4; ++g) { \
      const int o = KVOFF(4 * (T) + g + 4); \
      _Pragma("unroll") for (int i = 0; i < 4; ++i) { \
        v4u& kk = a[g * 4 + i]; \
        float d = dot2(kk[0], qpk[0], 0.f); d = dot2(kk[1], qpk[1], d); d = dot2(kk[2], qpk[2], d); d = dot2(kk[3], qpk[3], d); \
        asm volatile("" : "+v"(d)); \
        xs[g * 4 + i] = d; \
        SB(); \
        kk = __builtin_amdgcn_raw_buffer_load_b128(RN, kvoff, o + i * 1024, AUX_NT); \
        SB(); \
      } \
      LD_O(4 * (T) + g) \
    } \
    PACE_OUT() \
    TR_STEP(8, XOR8, 8) TR_STEP(4, XOR7, 4) TR_STEP(2, XOR2, 2) TR_STEP(1, XOR1, 1) \
    const float sc = xs[0] * (SCALE * LOG2E); \
    prow[16 * (T) + c] = sc; \
    mx = fmaxf(mx, sc); \
    if (tid == 64 * (NW - 1)) __hip_atomic_store((lu32*)&lds[L_SC + 3], (unsigned)(T) + 1u, RLX_WG); }
  KSTEP(0, rk) KSTEP(1, rk) KSTEP(2, rk) KSTEP(3, rv)
  mx = xrow_max(row_max(mx));
  {
    float ls = 0.f;
#pragma unroll
    for (int T = 0; T < 4; ++T) { const float p = __builtin_amdgcn_exp2f(prow[16 * T + c] - mx); prow[16 * T + c] = p; ls += p; }
    ls = xrow_sum(row_sum(ls));
    if (lane == 0) { lds[L_M + w] = mx; lds[L_L + w] = ls; lds_arrive(&lds[L_SC + 2]); }
  }
  LDS_DONE();
  SB();
#define WPERM(p) { _Pragma("unroll") for (int k = 0; k < 4; ++k) { const unsigned lo_ = __builtin_amdgcn_perm(wt[2 * (p) + 1][k], wt[2 * (p)][k], 0x05040100u), \
      hi_ = __builtin_amdgcn_perm(wt[2 * (p) + 1][k], wt[2 * (p)][k], 0x07060302u); wt[2 * (p)][k] = lo_; wt[2 * (p) + 1][k] = hi_; } \
      asm volatile("" : "+v"(wt[2 * (p)]), "+v"(wt[2 * (p) + 1])); }
  WPERM(0) WPERM(1) WPERM(2) WPERM(3) WPERM(4) WPERM(5)
  SB();
  float oa[8];
#pragma unroll
  for (int k = 0; k < 8; ++k) oa[k] = 0.f;
#pragma unroll 1
  for (int T = 0; T < 4; ++T) {
    PACE_IN(4 + T)
#pragma unroll
    for (int g = 0; g < 4; ++g) {
      const int un = 4 * T + g + 4;
      const int o = un < 16 ? KVOFF(un) : OOB;
      const f32x4 p4 = *(const f32x4*)&prow[16 * T + 4 * g];
#pragma unroll
      for (int i = 0; i < 4; ++i) {
        v4u& vv = a[g * 4 + i];
#pragma unroll
        for (int k = 0; k < 4; ++k) { oa[2 * k] = fmaf(p4[i], bflo(vv[k]), oa[2 * k]); oa[2 * k + 1] = fmaf(p4[i], bfhi(vv[k]), oa[2 * k + 1]); }
        KEEP8(oa);
        SB();
        vv = __builtin_amdgcn_raw_buffer_load_b128(rv, kvoff, o + i * 1024, AUX_NT);
        SB();
      }
    }
    PACE_OUT()
    if (tid == 64 * (NW - 1)) __hip_atomic_store((lu32*)&lds[L_SC + 3], (unsigned)T + 5u, RLX_WG);
  }
  WPERM(6) WPERM(7)
#pragma unroll
  for (int k = 0; k < 8; ++k) oa[k] = xrow_sum(oa[k]);
  if (r == 0) {
    *(f32x4*)&lds[L_OP + w * 128 + c * 8] = f32x4{oa[0], oa[1], oa[2], oa[3]};
    *(f32x4*)&lds[L_OP + w * 128 + c * 8 + 4] = f32x4{oa[4], oa[5], oa[6], oa[7]};
  }
  BAR();
  float od = 0.f;
#pragma unroll
  for (int ww = 0; ww < NW; ++ww) od = fmaf(lds[L_F + ww], lds[L_OP + ww * 128 + 16 * w + ((c + 2 * m8) & 15)], od);
  const unsigned opk = pk_bf16(od, dpp<XOR1>(od));
  float ya[8];
#pragma unroll
  for (int k = 0; k < 8; ++k) ya[k] = 0.f;
#pragma unroll
  for (int p = 0; p < 8; ++p) {
    const unsigned ov = __builtin_amdgcn_readlane(opk, 2 * p);
#pragma unroll
    for (int k = 0; k < 4; ++k) { ya[2 * k] = dot2(wt[2 * p][k], ov, ya[2 * k]); ya[2 * k + 1] = dot2(wt[2 * p + 1][k], ov, ya[2 * k + 1]); }
  }
  *(f32x4*)&lds[L_YP + w * 512 + lane * 8] = f32x4{ya[0], ya[1], ya[2], ya[3]};
  *(f32x4*)&lds[L_YP + w * 512 + lane * 8 + 4] = f32x4{ya[4], ya[5], ya[6], ya[7]};
  BAR();
  if (w < 2) {
    f32x4 y = *(const f32x4*)&lds[L_YP + tid * 4];
#pragma unroll
    for (int ww = 1; ww < NW; ++ww) y += *(const f32x4*)&lds[L_YP + ww * 512 + tid * 4];
    const v4u yu = {canon(y[0]), canon(y[1]), canon(y[2]), canon(y[3])};
    __builtin_amdgcn_raw_buffer_store_b128(yu, rw, tid * 16, WS_Y + chunk * 2048, AUX_SC1);
  }
}
}

extern "C" void kernel_launch(void* const* d_in, const int* in_sizes, int n_in, void* d_out, int out_size, void* d_ws, size_t ws_size,
                              hipStream_t stream) {
  typedef const unsigned short* P;
  hipLaunchKernelGGL(k_main, dim3(NWG), dim3(NTHR), 0, stream, (P)d_in[1], (P)d_in[3], (P)d_in[0], (P)d_in[2], (P)d_in[4], (unsigned char*)d_ws,
                     (P)d_in[9], (unsigned short*)d_out, (P)d_in[6], (P)d_in[7], (P)d_in[8]);
}
```

```cpp
#include <hip/hip_runtime.h>
#include <hip/hip_bf16.h>
#include <cstdint>

namespace {
typedef unsigned v4u __attribute__((ext_vector_type(4)));
typedef unsigned v2u __attribute__((ext_vector_type(2)));
typedef float f32x4 __attribute__((ext_vector_type(4)));
typedef __bf16 bf16x2 __attribute__((ext_vector_type(2)));
#define GAS __attribute__((address_space(1)))
typedef GAS unsigned gu32;
typedef GAS unsigned long long gu64;
typedef __attribute__((address_space(3))) unsigned lu32;
#define RLX_AGENT __ATOMIC_RELAXED, __HIP_MEMORY_SCOPE_AGENT
#define RLX_WG __ATOMIC_RELAXED, __HIP_MEMORY_SCOPE_WORKGROUP

constexpr int NB = 4, DM = 512, HQ = 32, SK = 4096, DH = 128, FF = 1376;
constexpr int NWG = 256, NW = 8, NTHR = 64 * (NW + 1);
constexpr int NSL = 43;
constexpr int NGU = 88;
constexpr int RSW3 = 0x00020000;
constexpr int AUX_NT = 2, AUX_SC1 = 16;
constexpr int OOB = 0x40000000;
constexpr float LOG2E = 1.4426950408889634f;
constexpr float SCALE = 0.08838834764831845f;
constexpr unsigned SENT = 0xFFFFFFFFu;
constexpr unsigned SPIN_CAP = 1u << 18;
constexpr int K0 = 4;

constexpr int LS = 384;
constexpr int WS_R = 0;
constexpr int WS_E = 64 * LS;
constexpr int WS_ML = 128 * LS;
constexpr int WS_Y = (WS_ML + 128 * LS + 4095) & ~4095;
constexpr int WS_YM = WS_Y + NWG * 2048;
constexpr int WS_GU = WS_YM + NSL * 8192;
constexpr int WS_END = WS_GU + NGU * 1024;

constexpr int L_QP = 0;
constexpr int L_P = L_QP + 8 * 128;
constexpr int L_M = L_P + 8 * 256;
constexpr int L_L = L_M + 8;
constexpr int L_SC = L_L + 8;
constexpr int L_F = L_SC + 8;
constexpr int L_OP = L_F + 8;
constexpr int L_YP = L_OP + 8 * 128;
constexpr int L_XT = L_YP + 8 * 512;
constexpr int L_END = L_XT + 272 * 4;

__device__ __forceinline__ float dot2(unsigned a, unsigned b, float acc) {
  return __builtin_amdgcn_fdot2_f32_bf16(__builtin_bit_cast(bf16x2, a), __builtin_bit_cast(bf16x2, b), acc, false);
}
__device__ __forceinline__ float bflo(unsigned u) { return __builtin_bit_cast(float, u << 16); }
__device__ __forceinline__ float bfhi(unsigned u) { return __builtin_bit_cast(float, u & 0xffff0000u); }
__device__ __forceinline__ unsigned pk_bf16(float lo, float hi) {
  bf16x2 r; r.x = (__bf16)lo; r.y = (__bf16)hi; return __builtin_bit_cast(unsigned, r);
}
__device__ __forceinline__ float rbf(float x) { return (float)(__bf16)x; }
template <int CTRL> __device__ __forceinline__ float dpp(float x) {
  return __builtin_bit_cast(float, __builtin_amdgcn_mov_dpp(__builtin_bit_cast(int, x), CTRL, 0xf, 0xf, true));
}
constexpr int XOR1 = 0xB1, XOR2 = 0x4E, XOR7 = 0x141, XOR8 = 0x128;
__device__ __forceinline__ float xrow_sum(float x) {
  auto s = __builtin_amdgcn_permlane16_swap(__float_as_uint(x), __float_as_uint(x), false, false);
  x = __uint_as_float(s[0]) + __uint_as_float(s[1]);
  auto t = __builtin_amdgcn_permlane32_swap(__float_as_uint(x), __float_as_uint(x), false, false);
  return __uint_as_float(t[0]) + __uint_as_float(t[1]);
}
__device__ __forceinline__ float xrow_max(float x) {
  auto s = __builtin_amdgcn_permlane16_swap(__float_as_uint(x), __float_as_uint(x), false, false);
  x = fmaxf(__uint_as_float(s[0]), __uint_as_float(s[1]));
  auto t = __builtin_amdgcn_permlane32_swap(__float_as_uint(x), __float_as_uint(x), false, false);
  return fmaxf(__uint_as_float(t[0]), __uint_as_float(t[1]));
}
__device__ __forceinline__ float row_sum(float x) { x += dpp<XOR1>(x); x += dpp<XOR2>(x); x += dpp<XOR7>(x); x += dpp<XOR8>(x); return x; }
__device__ __forceinline__ float row_max(float x) { x = fmaxf(x, dpp<XOR1>(x)); x = fmaxf(x, dpp<XOR2>(x)); x = fmaxf(x, dpp<XOR7>(x)); x = fmaxf(x, dpp<XOR8>(x)); return x; }
__device__ __forceinline__ float sigmoidf_(float x) { return 1.0f / (1.0f + __expf(-x)); }
__device__ __forceinline__ unsigned canon(float v) { const unsigned u = __float_as_uint(v); return u == SENT ? 0x7FC00000u : u; }
#define SB() __builtin_amdgcn_sched_barrier(0)
#define POLL_FENCE() asm volatile("" ::: "memory")
#define DRAIN() asm volatile("s_waitcnt vmcnt(0)" ::: "memory")
#define LDS_DONE() asm volatile("s_waitcnt lgkmcnt(0)" ::: "memory")
#define BAR() do { LDS_DONE(); __builtin_amdgcn_s_barrier(); asm volatile("" ::: "memory"); } while (0)
#define KEEP8(a) asm volatile("" : "+v"(a[0]), "+v"(a[1]), "+v"(a[2]), "+v"(a[3]), "+v"(a[4]), "+v"(a[5]), "+v"(a[6]), "+v"(a[7]))
#define TR_STEP(HALF, CTRL, BIT) _Pragma("unroll") for (int hh = 0; hh < (HALF); ++hh) { \
      const float lo_ = xs[hh], hi_ = xs[hh + (HALF)]; const float t1_ = lo_ + dpp<CTRL>(lo_), t2_ = hi_ + dpp<CTRL>(hi_); \
      xs[hh] = (c & (BIT)) ? t2_ : t1_; }

__device__ __forceinline__ void lds_arrive(float* p) { LDS_DONE(); __hip_atomic_fetch_add((lu32*)p, 1u, RLX_WG); }
__device__ __forceinline__ void lds_wait(float* p, unsigned n) {
#pragma unroll 1
  for (unsigned spin = 0; spin < (SPIN_CAP << 4); ++spin) {
    if (__builtin_amdgcn_readfirstlane(__hip_atomic_load((lu32*)p, RLX_WG)) >= n) break;
    __builtin_amdgcn_s_sleep(1);
  }
  asm volatile("" ::: "memory");
}

__global__ void __launch_bounds__(NTHR, 3)
k_main(const unsigned short* __restrict__ kc, const unsigned short* __restrict__ wq, const unsigned short* __restrict__ x,
       const unsigned short* __restrict__ vc, const unsigned short* __restrict__ wo, unsigned char* __restrict__ ws,
       const unsigned short* __restrict__ gw, unsigned short* __restrict__ out,
       const unsigned short* __restrict__ wg, const unsigned short* __restrict__ wu, const unsigned short* __restrict__ wd) {
  __shared__ __attribute__((aligned(16))) float lds[L_END];
  asm volatile("" :: "s"(x), "s"(kc), "s"(vc), "s"(wq), "s"(wo), "s"(ws), "s"(gw), "s"(out), "s"(wg), "s"(wu), "s"(wd));
  const int tid = threadIdx.x, lane = tid & 63, w = __builtin_amdgcn_readfirstlane(tid >> 6), r = lane >> 4, c = lane & 15;
  const int bid = blockIdx.x, xg = bid & 7, j = bid >> 3;
  const int h = xg * 4 + (j >> 3), b = (j >> 1) & 3, s = j & 1, m8 = j & 7;
  const int chunk = (b * HQ + h) * 2 + s;
  const __amdgpu_buffer_rsrc_t rx = __builtin_amdgcn_make_buffer_rsrc((void*)x, 0, NB * DM * 2, RSW3);
  const __amdgpu_buffer_rsrc_t rw = __builtin_amdgcn_make_buffer_rsrc((void*)ws, 0, WS_END, RSW3);

  if (w == NW) {
    gu32* R = (gu32*)(ws + WS_R + (chunk >> 2) * LS) + (chunk & 3);
    gu32* E = (gu32*)(ws + WS_E + (chunk >> 2) * LS) + (chunk & 3);
    gu64* ML = (gu64*)(ws + WS_ML + (chunk >> 1) * LS);
    __builtin_amdgcn_s_setprio(3);
    if (lane < 3) ((unsigned*)lds)[L_SC + 1 + lane] = 0u;
    const int rho = (bid & 1) ? (bid < 8 ? 127 + (bid >> 1) : 999) : (bid >> 1) - 1;
    const bool gu = (unsigned)rho < (unsigned)NGU, dn = rho >= NGU && rho < NGU + NSL;
    const int sl = rho - NGU;
    const unsigned r_old = __hip_atomic_load(R, RLX_AGENT), e_old = __hip_atomic_load(E, RLX_AGENT);
    const v4u rfirst = __builtin_amdgcn_raw_buffer_load_b128(rw, lane * LS, bid == 0 ? WS_R : OOB, AUX_SC1);
    v4u xv[4];
#pragma unroll
    for (int bb = 0; bb < 4; ++bb) xv[bb] = __builtin_amdgcn_raw_buffer_load_b128(rx, lane * 16, bb * DM * 2, 0);
    const v4u gv = *(const GAS v4u*)((const GAS unsigned char*)gw + lane * 16);
    const v4u sent4 = {SENT, SENT, SENT, SENT};
    __builtin_amdgcn_raw_buffer_store_b128(sent4, rw, lane * 16, WS_Y + chunk * 2048, AUX_SC1);
    __builtin_amdgcn_raw_buffer_store_b128(sent4, rw, lane * 16, WS_Y + chunk * 2048 + 1024, AUX_SC1);
    if (dn) {
#pragma unroll 1
      for (int i = 0; i < 8; ++i) __builtin_amdgcn_raw_buffer_store_b128(sent4, rw, lane * 16, WS_YM + sl * 8192 + i * 1024, AUX_SC1);
    }
    if (gu) __builtin_amdgcn_raw_buffer_store_b128(sent4, rw, lane * 16, WS_GU + rho * 1024, AUX_SC1);
    if (lane == 0) __hip_atomic_store(ML + s, ~0ull, RLX_AGENT);
    BAR();
    __builtin_amdgcn_s_setprio(0);
    const __amdgpu_buffer_rsrc_t rm = __builtin_amdgcn_make_buffer_rsrc((void*)(dn ? wd : (rho & 2) ? wu : wg), 0, (gu || dn) ? DM * FF * 2 : 0, RSW3);
    const int rowb = dn ? DM * 2 : FF * 2;
    const int mvo = dn ? lane * 16 : (lane >> 3) * 2 * (FF * 2) + (lane & 7) * 16;
    const int mso = dn ? sl * 32 * (DM * 2) : (rho & 1) * 256 * (FF * 2) + (rho >> 2) * 128, istep = dn ? 2 * (DM * 2) : 16 * (FF * 2);
    v4u m0[8], m1[8];
    if (gu || dn) {
#pragma unroll
      for (int i = 0; i < 8; ++i) {
        m0[i] = __builtin_amdgcn_raw_buffer_load_b128(rm, mvo, mso + i * istep, AUX_NT); SB();
        m1[i] = __builtin_amdgcn_raw_buffer_load_b128(rm, mvo, mso + i * istep + rowb, AUX_NT); SB();
      }
    }
    __builtin_amdgcn_s_setprio(3);
    BAR();
    unsigned nonce = (r_old & ~3u) + 4u; if (e_old - nonce < 4u) nonce += 4u;
    float gate_b;
    {
      const v4u xb = b == 0 ? xv[0] : b == 1 ? xv[1] : b == 2 ? xv[2] : xv[3];
      float gd = 0.f;
#pragma unroll
      for (int k = 0; k < 4; ++k) gd += rbf(bflo(xb[k]) * bflo(gv[k])) + rbf(bfhi(xb[k]) * bfhi(gv[k]));
      gate_b = rbf(sigmoidf_(rbf(xrow_sum(row_sum(gd)))));
    }
#pragma unroll
    for (int k = 0; k < 4; ++k) *(v4u*)&lds[L_XT + (4 * lane + k) * 4] = v4u{xv[0][k], xv[1][k], xv[2][k], xv[3][k]};
    DRAIN();
    if (lane == 0) __hip_atomic_store(R, nonce, RLX_AGENT);
    if (bid == 0) {
      unsigned seen = 0;
#pragma unroll 1
      for (unsigned spin = 0; spin < SPIN_CAP; ++spin) {
        POLL_FENCE();
        __builtin_amdgcn_s_sleep(40);
        const v4u rq = __builtin_amdgcn_raw_buffer_load_b128(rw, lane * LS, WS_R, AUX_SC1);
#pragma unroll
        for (int k = 0; k < 4; ++k) seen |= (rq[k] != rfirst[k]) ? 1u << k : 0u;
        if (__all(seen == 15u)) {
          __builtin_amdgcn_raw_buffer_store_b128((rq & ~3u) + 2u, rw, lane * LS, WS_E, AUX_SC1);
          break;
        }
        if (spin >= 2) {
#pragma unroll
          for (int k = 0; k < 4; ++k) if (!(seen >> k & 1u)) __hip_atomic_store((gu32*)(ws + WS_E + lane * LS) + k, rq[k], RLX_AGENT);
        }
      }
    }
#define WAIT_GO() do { _Pragma("unroll 1") for (unsigned spin = 0; spin < SPIN_CAP; ++spin) { POLL_FENCE(); \
        const unsigned e_ = __builtin_amdgcn_readfirstlane(__hip_atomic_load(E, RLX_AGENT)); \
        if (e_ == nonce + 2u) break; \
        if (e_ == nonce && lane == 0) __hip_atomic_store(R, nonce + 1u, RLX_AGENT); \
        __builtin_amdgcn_s_sleep(32); } } while (0)
    if (gu || dn) {
      if (dn) {
        lds_wait(&lds[L_SC + 3], 2);
        WAIT_GO();
        v2u pv[4];
        const int go = WS_GU + (sl >> 1) * 4096 + (sl & 1) * 128, gvo = r * 256 + c * 8;
#pragma unroll 1
        for (unsigned spin = 0; spin < SPIN_CAP; ++spin) {
          POLL_FENCE();
          bool ok = true;
#pragma unroll
          for (int k = 0; k < 4; ++k) { pv[k] = __builtin_amdgcn_raw_buffer_load_b64(rw, gvo, go + k * 1024, AUX_SC1); }
#pragma unroll
          for (int k = 0; k < 4; ++k) ok = ok && pv[k][0] != SENT && pv[k][1] != SENT;
          if (__all(ok)) break;
          __builtin_amdgcn_s_sleep(32);
        }
        const float g0 = __uint_as_float(pv[0][0]) + __uint_as_float(pv[1][0]), g1 = __uint_as_float(pv[0][1]) + __uint_as_float(pv[1][1]);
        const float u0 = __uint_as_float(pv[2][0]) + __uint_as_float(pv[3][0]), u1 = __uint_as_float(pv[2][1]) + __uint_as_float(pv[3][1]);
        ((unsigned*)lds)[L_XT + (256 + c) * 4 + r] = pk_bf16(rbf(sigmoidf_(rbf(g0))) * rbf(u0), rbf(sigmoidf_(rbf(g1))) * rbf(u1));
        LDS_DONE();
      }
      const float* xt = &lds[L_XT + (dn ? 256 * 4 : ((rho & 1) * 128 + (lane >> 3)) * 4)];
      const int xstep = dn ? 4 : 32;
      float acc[4][8];
#pragma unroll
      for (int bb = 0; bb < 4; ++bb)
#pragma unroll
        for (int k = 0; k < 8; ++k) acc[bb][k] = 0.f;
#pragma unroll 1
      for (int I0 = 0; I0 < 16; I0 += 8) {
#pragma unroll
        for (int i = 0; i < 8; ++i) {
          __builtin_amdgcn_s_setprio(0);
          const v4u xp = *(const v4u*)(xt + (I0 + i) * xstep);
#pragma unroll
          for (int k = 0; k < 4; ++k) {
            const unsigned lo = __builtin_amdgcn_perm(m1[i][k], m0[i][k], 0x05040100u), hi = __builtin_amdgcn_perm(m1[i][k], m0[i][k], 0x07060302u);
#pragma unroll
            for (int bb = 0; bb < 4; ++bb) { acc[bb][2 * k] = dot2(lo, xp[bb], acc[bb][2 * k]); acc[bb][2 * k + 1] = dot2(hi, xp[bb], acc[bb][2 * k + 1]); }
          }
          KEEP8(acc[0]); KEEP8(acc[1]); KEEP8(acc[2]); KEEP8(acc[3]);
          SB();
          __builtin_amdgcn_s_setprio(3);
          const int o = I0 == 0 ? mso + (8 + i) * istep : OOB;
          m0[i] = __builtin_amdgcn_raw_buffer_load_b128(rm, mvo, o, AUX_NT); SB();
          m1[i] = __builtin_amdgcn_raw_buffer_load_b128(rm, mvo, o + rowb, AUX_NT); SB();
        }
      }
      if (!dn) {
#pragma unroll
        for (int bb = 0; bb < 4; ++bb) {
          *(f32x4*)&lds[L_YP + lane * 32 + bb * 8] = f32x4{acc[bb][0], acc[bb][1], acc[bb][2], acc[bb][3]};
          *(f32x4*)&lds[L_YP + lane * 32 + bb * 8 + 4] = f32x4{acc[bb][4], acc[bb][5], acc[bb][6], acc[bb][7]};
        }
        LDS_DONE();
        f32x4 g4 = {0.f, 0.f, 0.f, 0.f};
        const float* sp = &lds[L_YP + (c >> 1) * 32 + r * 8 + (c & 1) * 4];
#pragma unroll
        for (int rg = 0; rg < 8; ++rg) g4 += *(const f32x4*)(sp + rg * 256);
        __builtin_amdgcn_raw_buffer_store_b128(v4u{canon(g4[0]), canon(g4[1]), canon(g4[2]), canon(g4[3])}, rw, lane * 16, WS_GU + rho * 1024, AUX_SC1);
      } else {
#pragma unroll
        for (int bb = 0; bb < 4; ++bb) {
          const v4u y0 = {canon(acc[bb][0]), canon(acc[bb][1]), canon(acc[bb][2]), canon(acc[bb][3])};
          const v4u y1 = {canon(acc[bb][4]), canon(acc[bb][5]), canon(acc[bb][6]), canon(acc[bb][7])};
          __builtin_amdgcn_raw_buffer_store_b128(y0, rw, lane * 32, WS_YM + sl * 8192 + bb * 2048, AUX_SC1);
          __builtin_amdgcn_raw_buffer_store_b128(y1, rw, lane * 32 + 16, WS_YM + sl * 8192 + bb * 2048, AUX_SC1);
        }
      }
    }
    lds_wait(&lds[L_SC + 2], NW);
    const float mw = lds[L_M + (lane & 7)];
    float M = fmaxf(mw, dpp<XOR1>(mw)); M = fmaxf(M, dpp<XOR2>(M)); M = fmaxf(M, dpp<XOR7>(M));
    const float ew = __builtin_amdgcn_exp2f(mw - M);
    float L = ew * lds[L_L + (lane & 7)]; L += dpp<XOR1>(L); L += dpp<XOR2>(L); L += dpp<XOR7>(L);
    if (lane == 0) __hip_atomic_store(ML + s, ((unsigned long long)canon(L) << 32) | canon(M), RLX_AGENT);
    if (!dn) WAIT_GO();
    lds_wait(&lds[L_SC + 3], 6);
    unsigned long long pm = ~0ull;
#pragma unroll 1
    for (unsigned spin = 0; spin < SPIN_CAP; ++spin) {
      POLL_FENCE();
      pm = __hip_atomic_load(ML + (s ^ 1), RLX_AGENT);
      if (__builtin_amdgcn_readfirstlane((unsigned)pm) != SENT) break;
      __builtin_amdgcn_s_sleep(32);
    }
    {
      const float M2 = __uint_as_float((unsigned)pm), L2 = __uint_as_float((unsigned)(pm >> 32));
      const float Mx = fmaxf(M, M2), e1 = __builtin_amdgcn_exp2f(M - Mx), e2 = __builtin_amdgcn_exp2f(M2 - Mx);
      if (lane < 8) lds[L_F + lane] = ew * e1 / (e1 * L + e2 * L2);
    }
    const int u = chunk & 63;
    float mix = 0.f;
#pragma unroll 1
    for (int round = 0; round < 2; ++round) {
      const int yo = round ? WS_Y + (b * 64) * 2048 + u * 32 : WS_YM + b * 2048 + u * 32;
      const int vo = round ? lane * 2048 : lane * 8192;
      v4u y0 = {0u, 0u, 0u, 0u}, y1 = y0;
      bool ok = !round && lane >= NSL;
      BAR();
#pragma unroll 1
      for (unsigned spin = 0; spin < SPIN_CAP; ++spin) {
        POLL_FENCE();
        if (!ok) {
          y0 = __builtin_amdgcn_raw_buffer_load_b128(rw, vo, yo, AUX_SC1);
          y1 = __builtin_amdgcn_raw_buffer_load_b128(rw, vo, yo + 16, AUX_SC1);
          ok = y0[0] != SENT && y0[1] != SENT && y0[2] != SENT && y0[3] != SENT && y1[0] != SENT && y1[1] != SENT && y1[2] != SENT && y1[3] != SENT;
        }
        if (__all(ok)) break;
        __builtin_amdgcn_s_sleep(2);
      }
      float xs[8];
#pragma unroll
      for (int k = 0; k < 4; ++k) { xs[k] = __uint_as_float(y0[k]); xs[4 + k] = __uint_as_float(y1[k]); }
      TR_STEP(4, XOR8, 8) TR_STEP(2, XOR7, 4) TR_STEP(1, XOR2, 2)
      float o = xs[0]; o += dpp<XOR1>(o); o = xrow_sum(o);
      mix += rbf((round ? gate_b : rbf(1.0f - gate_b)) * rbf(o));
    }
    if (lane < 16 && !(lane & 1)) out[b * DM + u * 8 + (lane >> 1)] = __builtin_bit_cast(unsigned short, (__bf16)mix);
    return;
  }

  const __amdgpu_buffer_rsrc_t rk = __builtin_amdgcn_make_buffer_rsrc((void*)kc, 0, NB * HQ * SK * DH * 2, RSW3);
  const __amdgpu_buffer_rsrc_t rv = __builtin_amdgcn_make_buffer_rsrc((void*)vc, 0, NB * HQ * SK * DH * 2, RSW3);
  const __amdgpu_buffer_rsrc_t rq = __builtin_amdgcn_make_buffer_rsrc((void*)wq, 0, DM * HQ * DH * 2, RSW3);
  const __amdgpu_buffer_rsrc_t ro = __builtin_amdgcn_make_buffer_rsrc((void*)wo, 0, DM * HQ * DH * 2, RSW3);

  const int kvoff = lane * 16;
  const int kvbase = chunk * (512 * 1024) + w * 4096;
  const int r0 = chunk * 4;
#define KVOFF(t) (kvbase + ((((t) + r0) & 15) << 15))
  v4u wq_[16], wt[16];
  v4u a[16];
  unsigned xd[8];
  const int oso = (h * DH + 16 * w) * (DM * 2);
  const int qvoff = r * 2 * (HQ * DH * 2) + c * 16, qso = 8 * w * (HQ * DH * 2) + h * DH * 2;
  const int rot = 2 * b;
#define KLOAD(n) { a[n] = __builtin_amdgcn_raw_buffer_load_b128(rk, kvoff, KVOFF((n) >> 2) + ((n) & 3) * 1024, AUX_NT); SB(); }
#define LD_O(i) { wt[i] = __builtin_amdgcn_raw_buffer_load_b128(ro, kvoff, oso + (((i) + 2 * m8) & 15) * (DM * 2), 0); SB(); }
#define LD_Q(i) { wq_[i] = __builtin_amdgcn_raw_buffer_load_b128(rq, qvoff, qso + (64 * ((((i) >> 1) + rot) & 7) + ((i) & 1)) * (HQ * DH * 2), 0); SB(); }
#define LD_X() { _Pragma("unroll") for (int k = 0; k < 8; ++k) xd[k] = __builtin_amdgcn_raw_buffer_load_b32(rx, r * 4, (b * DM + 64 * ((k + rot) & 7) + 8 * w) * 2, 0); SB(); }
  const bool early = s == 0;
  if (early) {
#pragma unroll
    for (int i = 0; i < 12; ++i) LD_Q(i)
    LD_X()
#pragma unroll
    for (int n = 0; n < K0; ++n) KLOAD(n)
#pragma unroll
    for (int i = 12; i < 16; ++i) LD_Q(i)
  } else {
#pragma unroll
    for (int n = 0; n < 16; ++n) KLOAD(n)
  }
  BAR();
  SB();
  if (!early) {
    LD_X()
#pragma unroll
    for (int i = 0; i < 16; ++i) LD_Q(i)
  }
  {
    float qa[8];
#pragma unroll
    for (int k = 0; k < 8; ++k) qa[k] = 0.f;
#pragma unroll
    for (int i = 0; i < 16; ++i) {
      const unsigned xx = xd[i >> 1];
      const unsigned xl = (i & 1) ? (xx >> 16) : (xx & 0xffffu), xh = (i & 1) ? (xx & 0xffff0000u) : (xx << 16);
#pragma unroll
      for (int k = 0; k < 4; ++k) { qa[2 * k] = dot2(wq_[i][k], xl, qa[2 * k]); qa[2 * k + 1] = dot2(wq_[i][k], xh, qa[2 * k + 1]); }
    }
#pragma unroll
    for (int k = 0; k < 8; ++k) qa[k] = xrow_sum(qa[k]);
    KEEP8(qa);
    if (r == 0) {
      *(f32x4*)&lds[L_QP + w * 128 + c * 8] = f32x4{qa[0], qa[1], qa[2], qa[3]};
      *(f32x4*)&lds[L_QP + w * 128 + c * 8 + 4] = f32x4{qa[4], qa[5], qa[6], qa[7]};
    }
    SB();
  }
  if (early) {
#pragma unroll
    for (int n = K0; n < 16; ++n) KLOAD(n)
  }
  BAR();
  unsigned qpk[4];
  {
    f32x4 q0 = {0.f, 0.f, 0.f, 0.f}, q1 = q0;
#pragma unroll
    for (int ww = 0; ww < NW; ++ww) { q0 += *(const f32x4*)&lds[L_QP + ww * 128 + c * 8]; q1 += *(const f32x4*)&lds[L_QP + ww * 128 + c * 8 + 4]; }
    qpk[0] = pk_bf16(q0[0], q0[1]); qpk[1] = pk_bf16(q0[2], q0[3]); qpk[2] = pk_bf16(q1[0], q1[1]); qpk[3] = pk_bf16(q1[2], q1[3]);
    asm volatile("" : "+v"(qpk[0]), "+v"(qpk[1]), "+v"(qpk[2]), "+v"(qpk[3]));
    SB();
  }
  float* prow = &lds[L_P + (w * 4 + r) * 64];
  float mx = -INFINITY;
#define PACE_IN(st) { if ((st) >= 1) lds_wait(&lds[L_SC + 1], NW * (st)); }
#define PACE_OUT() { if (lane == 0) __hip_atomic_fetch_add((lu32*)&lds[L_SC + 1], 1u, RLX_WG); }
#define KSTEP(T, RN) { \
    PACE_IN(T) \
    float xs[16]; \
    _Pragma("unroll") for (int g = 0; g < 4; ++g) { \
      const int o = KVOFF(4 * (T) + g + 4); \
      _Pragma("unroll") for (int i = 0; i < 4; ++i) { \
        v4u& kk = a[g * 4 + i]; \
        float d = dot2(kk[0], qpk[0], 0.f); d = dot2(kk[1], qpk[1], d); d = dot2(kk[2], qpk[2], d); d = dot2(kk[3], qpk[3], d); \
        asm volatile("" : "+v"(d)); \
        xs[g * 4 + i] = d; \
        SB(); \
        kk = __builtin_amdgcn_raw_buffer_load_b128(RN, kvoff, o + i * 1024, AUX_NT); \
        SB(); \
      } \
      LD_O(4 * (T) + g) \
    } \
    PACE_OUT() \
    TR_STEP(8, XOR8, 8) TR_STEP(4, XOR7, 4) TR_STEP(2, XOR2, 2) TR_STEP(1, XOR1, 1) \
    const float sc = rbf(rbf(xs[0]) * SCALE) * LOG2E; \
    prow[16 * (T) + c] = sc; \
    mx = fmaxf(mx, sc); \
    if (tid == 64 * (NW - 1)) __hip_atomic_store((lu32*)&lds[L_SC + 3], (unsigned)(T) + 1u, RLX_WG); }
  KSTEP(0, rk) KSTEP(1, rk) KSTEP(2, rk) KSTEP(3, rv)
  mx = xrow_max(row_max(mx));
  {
    float ls = 0.f;
#pragma unroll
    for (int T = 0; T < 4; ++T) { const float p = __builtin_amdgcn_exp2f(prow[16 * T + c] - mx); prow[16 * T + c] = p; ls += p; }
    ls = xrow_sum(row_sum(ls));
    if (lane == 0) { lds[L_M + w] = mx; lds[L_L + w] = ls; lds_arrive(&lds[L_SC + 2]); }
  }
  LDS_DONE();
  SB();
#define WPERM(p) { _Pragma("unroll") for (int k = 0; k < 4; ++k) { const unsigned lo_ = __builtin_amdgcn_perm(wt[2 * (p) + 1][k], wt[2 * (p)][k], 0x05040100u), \
      hi_ = __builtin_amdgcn_perm(wt[2 * (p) + 1][k], wt[2 * (p)][k], 0x07060302u); wt[2 * (p)][k] = lo_; wt[2 * (p) + 1][k] = hi_; } \
      asm volatile("" : "+v"(wt[2 * (p)]), "+v"(wt[2 * (p) + 1])); }
  WPERM(0) WPERM(1) WPERM(2) WPERM(3) WPERM(4) WPERM(5)
  SB();
  float oa[8];
#pragma unroll
  for (int k = 0; k < 8; ++k) oa[k] = 0.f;
#pragma unroll 1
  for (int T = 0; T < 4; ++T) {
    PACE_IN(4 + T)
#pragma unroll
    for (int g = 0; g < 4; ++g) {
      const int un = 4 * T + g + 4;
      const int o = un < 16 ? KVOFF(un) : OOB;
      const f32x4 p4 = *(const f32x4*)&prow[16 * T + 4 * g];
#pragma unroll
      for (int i = 0; i < 4; ++i) {
        v4u& vv = a[g * 4 + i];
#pragma unroll
        for (int k = 0; k < 4; ++k) { oa[2 * k] = fmaf(p4[i], bflo(vv[k]), oa[2 * k]); oa[2 * k + 1] = fmaf(p4[i], bfhi(vv[k]), oa[2 * k + 1]); }
        KEEP8(oa);
        SB();
        vv = __builtin_amdgcn_raw_buffer_load_b128(rv, kvoff, o + i * 1024, AUX_NT);
        SB();
      }
    }
    PACE_OUT()
    if (tid == 64 * (NW - 1)) __hip_atomic_store((lu32*)&lds[L_SC + 3], (unsigned)T + 5u, RLX_WG);
  }
  WPERM(6) WPERM(7)
#pragma unroll
  for (int k = 0; k < 8; ++k) oa[k] = xrow_sum(oa[k]);
  if (r == 0) {
    *(f32x4*)&lds[L_OP + w * 128 + c * 8] = f32x4{oa[0], oa[1], oa[2], oa[3]};
    *(f32x4*)&lds[L_OP + w * 128 + c * 8 + 4] = f32x4{oa[4], oa[5], oa[6], oa[7]};
  }
  BAR();
  float od = 0.f;
#pragma unroll
  for (int ww = 0; ww < NW; ++ww) od = fmaf(lds[L_F + ww], lds[L_OP + ww * 128 + 16 * w + ((c + 2 * m8) & 15)], od);
  const unsigned opk = pk_bf16(od, dpp<XOR1>(od));
  float ya[8];
#pragma unroll
  for (int k = 0; k < 8; ++k) ya[k] = 0.f;
#pragma unroll
  for (int p = 0; p < 8; ++p) {
    const unsigned ov = __builtin_amdgcn_readlane(opk, 2 * p);
#pragma unroll
    for (int k = 0; k < 4; ++k) { ya[2 * k] = dot2(wt[2 * p][k], ov, ya[2 * k]); ya[2 * k + 1] = dot2(wt[2 * p + 1][k], ov, ya[2 * k + 1]); }
  }
  *(f32x4*)&lds[L_YP + w * 512 + lane * 8] = f32x4{ya[0], ya[1], ya[2], ya[3]};
  *(f32x4*)&lds[L_YP + w * 512 + lane * 8 + 4] = f32x4{ya[4], ya[5], ya[6], ya[7]};
  BAR();
  if (w < 2) {
    f32x4 y = *(const f32x4*)&lds[L_YP + tid * 4];
#pragma unroll
    for (int ww = 1; ww < NW; ++ww) y += *(const f32x4*)&lds[L_YP + ww * 512 + tid * 4];
    const v4u yu = {canon(y[0]), canon(y[1]), canon(y[2]), canon(y[3])};
    __builtin_amdgcn_raw_buffer_store_b128(yu, rw, tid * 16, WS_Y + chunk * 2048, AUX_SC1);
  }
}
}

extern "C" void kernel_launch(void* const* d_in, const int* in_sizes, int n_in, void* d_out, int out_size, void* d_ws, size_t ws_size,
                              hipStream_t stream) {
  typedef const unsigned short* P;
  hipLaunchKernelGGL(k_main, dim3(NWG), dim3(NTHR), 0, stream, (P)d_in[1], (P)d_in[3], (P)d_in[0], (P)d_in[2], (P)d_in[4], (unsigned char*)d_ws,
                     (P)d_in[9], (unsigned short*)d_out, (P)d_in[6], (P)d_in[7], (P)d_in[8]);
}
```

```cpp
#include <hip/hip_runtime.h>
#include <hip/hip_bf16.h>
#include <cstdint>

namespace {
typedef unsigned v4u __attribute__((ext_vector_type(4)));
typedef unsigned v2u __attribute__((ext_vector_type(2)));
typedef float f32x4 __attribute__((ext_vector_type(4)));
typedef __bf16 bf16x2 __attribute__((ext_vector_type(2)));
#define GAS __attribute__((address_space(1)))
typedef GAS unsigned gu32;
typedef GAS unsigned long long gu64;
typedef __attribute__((address_space(3))) unsigned lu32;
#define RLX_AGENT __ATOMIC_RELAXED, __HIP_MEMORY_SCOPE_AGENT
#define RLX_WG __ATOMIC_RELAXED, __HIP_MEMORY_SCOPE_WORKGROUP

constexpr int NB = 4, DM = 512, HQ = 32, SK = 4096, DH = 128, FF = 1376;
constexpr int NWG = 256, NW = 8, NTHR = 64 * (NW + 1);
constexpr int NDN = 48;
constexpr int NGU = 88;
constexpr int RSW3 = 0x00020000;
constexpr int AUX_NT = 2, AUX_SC1 = 16;
constexpr int OOB = 0x40000000;
constexpr float LOG2E = 1.4426950408889634f;
constexpr float SCALE = 0.08838834764831845f;
constexpr unsigned SENT = 0xFFFFFFFFu;
constexpr unsigned SPIN_CAP = 1u << 18;
constexpr int K0 = 4;

constexpr int LS = 384;
constexpr int WS_R = 0;
constexpr int WS_E = 64 * LS;
constexpr int WS_ML = 128 * LS;
constexpr int WS_Y = (WS_ML + 128 * LS + 4095) & ~4095;
constexpr int WS_GU = WS_Y + NWG * 2048;
constexpr int WS_H = WS_GU + (NGU + NDN) * 1024;
constexpr int WS_END = WS_H + 704 * 16;

constexpr int L_QP = 0;
constexpr int L_P = L_QP + 8 * 128;
constexpr int L_M = L_P + 8 * 256;
constexpr int L_L = L_M + 8;
constexpr int L_SC = L_L + 8;
constexpr int L_F = L_SC + 8;
constexpr int L_OP = L_F + 8;
constexpr int L_YP = L_OP + 8 * 128;
constexpr int L_XT = L_YP + 8 * 512;
constexpr int L_END = L_XT + 256 * 4;

__device__ __forceinline__ float dot2(unsigned a, unsigned b, float acc) {
  return __builtin_amdgcn_fdot2_f32_bf16(__builtin_bit_cast(bf16x2, a), __builtin_bit_cast(bf16x2, b), acc, false);
}
__device__ __forceinline__ float bflo(unsigned u) { return __builtin_bit_cast(float, u << 16); }
__device__ __forceinline__ float bfhi(unsigned u) { return __builtin_bit_cast(float, u & 0xffff0000u); }
__device__ __forceinline__ unsigned pk_bf16(float lo, float hi) {
  bf16x2 r; r.x = (__bf16)lo; r.y = (__bf16)hi; return __builtin_bit_cast(unsigned, r);
}
__device__ __forceinline__ float rbf(float x) { return (float)(__bf16)x; }
template <int CTRL> __device__ __forceinline__ float dpp(float x) {
  return __builtin_bit_cast(float, __builtin_amdgcn_mov_dpp(__builtin_bit_cast(int, x), CTRL, 0xf, 0xf, true));
}
constexpr int XOR1 = 0xB1, XOR2 = 0x4E, XOR7 = 0x141, XOR8 = 0x128;
__device__ __forceinline__ float xrow_sum(float x) {
  auto s = __builtin_amdgcn_permlane16_swap(__float_as_uint(x), __float_as_uint(x), false, false);
  x = __uint_as_float(s[0]) + __uint_as_float(s[1]);
  auto t = __builtin_amdgcn_permlane32_swap(__float_as_uint(x), __float_as_uint(x), false, false);
  return __uint_as_float(t[0]) + __uint_as_float(t[1]);
}
__device__ __forceinline__ float xrow_max(float x) {
  auto s = __builtin_amdgcn_permlane16_swap(__float_as_uint(x), __float_as_uint(x), false, false);
  x = fmaxf(__uint_as_float(s[0]), __uint_as_float(s[1]));
  auto t = __builtin_amdgcn_permlane32_swap(__float_as_uint(x), __float_as_uint(x), false, false);
  return fmaxf(__uint_as_float(t[0]), __uint_as_float(t[1]));
}
__device__ __forceinline__ float row_sum(float x) { x += dpp<XOR1>(x); x += dpp<XOR2>(x); x += dpp<XOR7>(x); x += dpp<XOR8>(x); return x; }
__device__ __forceinline__ float row_max(float x) { x = fmaxf(x, dpp<XOR1>(x)); x = fmaxf(x, dpp<XOR2>(x)); x = fmaxf(x, dpp<XOR7>(x)); x = fmaxf(x, dpp<XOR8>(x)); return x; }
__device__ __forceinline__ float sigmoidf_(float x) { return 1.0f / (1.0f + __expf(-x)); }
__device__ __forceinline__ unsigned canon(float v) { const unsigned u = __float_as_uint(v); return u == SENT ? 0x7FC00000u : u; }
#define SB() __builtin_amdgcn_sched_barrier(0)
#define POLL_FENCE() asm volatile("" ::: "memory")
#define DRAIN() asm volatile("s_waitcnt vmcnt(0)" ::: "memory")
#define LDS_DONE() asm volatile("s_waitcnt lgkmcnt(0)" ::: "memory")
#define BAR() do { LDS_DONE(); __builtin_amdgcn_s_barrier(); asm volatile("" ::: "memory"); } while (0)
#define KEEP8(a) asm volatile("" : "+v"(a[0]), "+v"(a[1]), "+v"(a[2]), "+v"(a[3]), "+v"(a[4]), "+v"(a[5]), "+v"(a[6]), "+v"(a[7]))
#define TR_STEP(HALF, CTRL, BIT) _Pragma("unroll") for (int hh = 0; hh < (HALF); ++hh) { \
      const float lo_ = xs[hh], hi_ = xs[hh + (HALF)]; const float t1_ = lo_ + dpp<CTRL>(lo_), t2_ = hi_ + dpp<CTRL>(hi_); \
      xs[hh] = (c & (BIT)) ? t2_ : t1_; }

__device__ __forceinline__ void lds_arrive(float* p) { LDS_DONE(); __hip_atomic_fetch_add((lu32*)p, 1u, RLX_WG); }
__device__ __forceinline__ void lds_wait(float* p, unsigned n) {
#pragma unroll 1
  for (unsigned spin = 0; spin < (SPIN_CAP << 4); ++spin) {
    if (__builtin_amdgcn_readfirstlane(__hip_atomic_load((lu32*)p, RLX_WG)) >= n) break;
    __builtin_amdgcn_s_sleep(1);
  }
  asm volatile("" ::: "memory");
}

__global__ void __launch_bounds__(NTHR, 3)
k_main(const unsigned short* __restrict__ kc, const unsigned short* __restrict__ wq, const unsigned short* __restrict__ x,
       const unsigned short* __restrict__ vc, const unsigned short* __restrict__ wo, unsigned char* __restrict__ ws,
       const unsigned short* __restrict__ gw, unsigned short* __restrict__ out,
       const unsigned short* __restrict__ wg, const unsigned short* __restrict__ wu, const unsigned short* __restrict__ wd) {
  __shared__ __attribute__((aligned(16))) float lds[L_END];
  asm volatile("" :: "s"(x), "s"(kc), "s"(vc), "s"(wq), "s"(wo), "s"(ws), "s"(gw), "s"(out), "s"(wg), "s"(wu), "s"(wd));
  const int tid = threadIdx.x, lane = tid & 63, w = __builtin_amdgcn_readfirstlane(tid >> 6), r = lane >> 4, c = lane & 15;
  const int bid = blockIdx.x, xg = bid & 7, j = bid >> 3;
  const int h = xg * 4 + (j >> 3), b = (j >> 1) & 3, s = j & 1, m8 = j & 7;
  const int chunk = (b * HQ + h) * 2 + s;
  const __amdgpu_buffer_rsrc_t rx = __builtin_amdgcn_make_buffer_rsrc((void*)x, 0, NB * DM * 2, RSW3);
  const __amdgpu_buffer_rsrc_t rw = __builtin_amdgcn_make_buffer_rsrc((void*)ws, 0, WS_END, RSW3);

  if (w == NW) {
    gu32* R = (gu32*)(ws + WS_R + (chunk >> 2) * LS) + (chunk & 3);
    gu32* E = (gu32*)(ws + WS_E + (chunk >> 2) * LS) + (chunk & 3);
    gu64* ML = (gu64*)(ws + WS_ML + (chunk >> 1) * LS);
    __builtin_amdgcn_s_setprio(3);
    if (lane < 3) ((unsigned*)lds)[L_SC + 1 + lane] = 0u;
    const int rho = (bid & 1) ? (bid < 18 ? 127 + (bid >> 1) : 999) : (bid >> 1) - 1;
    const bool gu = (unsigned)rho < (unsigned)NGU, dn = rho >= NGU && rho < NGU + NDN, hs = gu && !(rho & 3);
    const int dd = rho - NGU;
    const unsigned r_old = __hip_atomic_load(R, RLX_AGENT), e_old = __hip_atomic_load(E, RLX_AGENT);
    const v4u rfirst = __builtin_amdgcn_raw_buffer_load_b128(rw, lane * LS, bid == 0 ? WS_R : OOB, AUX_SC1);
    v4u xv[4];
#pragma unroll
    for (int bb = 0; bb < 4; ++bb) xv[bb] = __builtin_amdgcn_raw_buffer_load_b128(rx, lane * 16, bb * DM * 2, 0);
    const v4u gv = *(const GAS v4u*)((const GAS unsigned char*)gw + lane * 16);
    const v4u sent4 = {SENT, SENT, SENT, SENT};
    __builtin_amdgcn_raw_buffer_store_b128(sent4, rw, lane * 16, WS_Y + chunk * 2048, AUX_SC1);
    __builtin_amdgcn_raw_buffer_store_b128(sent4, rw, lane * 16, WS_Y + chunk * 2048 + 1024, AUX_SC1);
    if (gu || dn) __builtin_amdgcn_raw_buffer_store_b128(sent4, rw, lane * 16, WS_GU + rho * 1024, AUX_SC1);
    if (hs && lane < 32) __builtin_amdgcn_raw_buffer_store_b128(sent4, rw, lane * 16, WS_H + (rho >> 2) * 512, AUX_SC1);
    if (lane == 0) __hip_atomic_store(ML + s, ~0ull, RLX_AGENT);
    BAR();
    __builtin_amdgcn_s_setprio(0);
    const __amdgpu_buffer_rsrc_t rm = __builtin_amdgcn_make_buffer_rsrc((void*)(dn ? wd : (rho & 2) ? wu : wg), 0, (gu || dn) ? DM * FF * 2 : 0, RSW3);
    const int rowb = dn ? DM * 2 : FF * 2;
    const int mvo = (lane >> 3) * 2 * rowb + (lane & 7) * 16;
    const int mso = (dn ? dd >> 3 : rho & 1) * 256 * rowb + (dn ? dd & 7 : rho >> 2) * 128, istep = 16 * rowb;
    v4u m0[8], m1[8];
    if (gu || dn) {
#pragma unroll
      for (int i = 0; i < 8; ++i) {
        m0[i] = __builtin_amdgcn_raw_buffer_load_b128(rm, mvo, mso + i * istep, 0); SB();
        m1[i] = __builtin_amdgcn_raw_buffer_load_b128(rm, mvo, mso + i * istep + rowb, 0); SB();
      }
    }
    __builtin_amdgcn_s_setprio(3);
    BAR();
    unsigned nonce = (r_old & ~3u) + 4u; if (e_old - nonce < 4u) nonce += 4u;
    float gate_b;
    {
      const v4u xb = b == 0 ? xv[0] : b == 1 ? xv[1] : b == 2 ? xv[2] : xv[3];
      float gd = 0.f;
#pragma unroll
      for (int k = 0; k < 4; ++k) gd += rbf(bflo(xb[k]) * bflo(gv[k])) + rbf(bfhi(xb[k]) * bfhi(gv[k]));
      gate_b = rbf(sigmoidf_(rbf(xrow_sum(row_sum(gd)))));
    }
#pragma unroll
    for (int k = 0; k < 4; ++k) *(v4u*)&lds[L_XT + (4 * lane + k) * 4] = v4u{xv[0][k], xv[1][k], xv[2][k], xv[3][k]};
    DRAIN();
    if (lane == 0) __hip_atomic_store(R, nonce, RLX_AGENT);
    if (bid == 0) {
      unsigned seen = 0;
#pragma unroll 1
      for (unsigned spin = 0; spin < SPIN_CAP; ++spin) {
        POLL_FENCE();
        __builtin_amdgcn_s_sleep(40);
        const v4u rq = __builtin_amdgcn_raw_buffer_load_b128(rw, lane * LS, WS_R, AUX_SC1);
#pragma unroll
        for (int k = 0; k < 4; ++k) seen |= (rq[k] != rfirst[k]) ? 1u << k : 0u;
        if (__all(seen == 15u)) {
          __builtin_amdgcn_raw_buffer_store_b128((rq & ~3u) + 2u, rw, lane * LS, WS_E, AUX_SC1);
          break;
        }
        if (spin >= 2) {
#pragma unroll
          for (int k = 0; k < 4; ++k) if (!(seen >> k & 1u)) __hip_atomic_store((gu32*)(ws + WS_E + lane * LS) + k, rq[k], RLX_AGENT);
        }
      }
    }
#define WAIT_GO() do { _Pragma("unroll 1") for (unsigned spin = 0; spin < SPIN_CAP; ++spin) { POLL_FENCE(); \
        const unsigned e_ = __builtin_amdgcn_readfirstlane(__hip_atomic_load(E, RLX_AGENT)); \
        if (e_ == nonce + 2u) break; \
        if (e_ == nonce && lane == 0) __hip_atomic_store(R, nonce + 1u, RLX_AGENT); \
        __builtin_amdgcn_s_sleep(32); } } while (0)
    if (gu || dn) {
      if (dn) {
        lds_wait(&lds[L_SC + 3], 3);
        WAIT_GO();
        v4u hv[2];
        const int ho = WS_H + (dd >> 3) * 2048;
#pragma unroll 1
        for (unsigned spin = 0; spin < SPIN_CAP; ++spin) {
          POLL_FENCE();
          bool ok = true;
#pragma unroll
          for (int k = 0; k < 2; ++k) hv[k] = __builtin_amdgcn_raw_buffer_load_b128(rw, lane * 16, ho + k * 1024, AUX_SC1);
#pragma unroll
          for (int k = 0; k < 2; ++k) ok = ok && hv[k][0] != SENT && hv[k][1] != SENT && hv[k][2] != SENT && hv[k][3] != SENT;
          if (__all(ok)) break;
          __builtin_amdgcn_s_sleep(32);
        }
        *(v4u*)&lds[L_XT + lane * 4] = hv[0];
        *(v4u*)&lds[L_XT + 256 + lane * 4] = hv[1];
        LDS_DONE();
      }
      const float* xt = &lds[L_XT + ((dn ? 0 : rho & 1) * 128 + (lane >> 3)) * 4];
      constexpr int xstep = 32;
      float acc[4][8];
#pragma unroll
      for (int bb = 0; bb < 4; ++bb)
#pragma unroll
        for (int k = 0; k < 8; ++k) acc[bb][k] = 0.f;
#pragma unroll 1
      for (int I0 = 0; I0 < 16; I0 += 8) {
#pragma unroll
        for (int i = 0; i < 8; ++i) {
          __builtin_amdgcn_s_setprio(0);
          const v4u xp = *(const v4u*)(xt + (I0 + i) * xstep);
#pragma unroll
          for (int k = 0; k < 4; ++k) {
            const unsigned lo = __builtin_amdgcn_perm(m1[i][k], m0[i][k], 0x05040100u), hi = __builtin_amdgcn_perm(m1[i][k], m0[i][k], 0x07060302u);
#pragma unroll
            for (int bb = 0; bb < 4; ++bb) { acc[bb][2 * k] = dot2(lo, xp[bb], acc[bb][2 * k]); acc[bb][2 * k + 1] = dot2(hi, xp[bb], acc[bb][2 * k + 1]); }
          }
          KEEP8(acc[0]); KEEP8(acc[1]); KEEP8(acc[2]); KEEP8(acc[3]);
          SB();
          __builtin_amdgcn_s_setprio(3);
          const int o = I0 == 0 ? mso + (8 + i) * istep : OOB;
          m0[i] = __builtin_amdgcn_raw_buffer_load_b128(rm, mvo, o, 0); SB();
          m1[i] = __builtin_amdgcn_raw_buffer_load_b128(rm, mvo, o + rowb, 0); SB();
        }
      }
#pragma unroll
      for (int bb = 0; bb < 4; ++bb) {
        *(f32x4*)&lds[L_YP + lane * 32 + bb * 8] = f32x4{acc[bb][0], acc[bb][1], acc[bb][2], acc[bb][3]};
        *(f32x4*)&lds[L_YP + lane * 32 + bb * 8 + 4] = f32x4{acc[bb][4], acc[bb][5], acc[bb][6], acc[bb][7]};
      }
      LDS_DONE();
      f32x4 g4 = {0.f, 0.f, 0.f, 0.f};
      const float* sp = &lds[L_YP + (c >> 1) * 32 + r * 8 + (c & 1) * 4];
#pragma unroll
      for (int rg = 0; rg < 8; ++rg) g4 += *(const f32x4*)(sp + rg * 256);
      __builtin_amdgcn_raw_buffer_store_b128(v4u{canon(g4[0]), canon(g4[1]), canon(g4[2]), canon(g4[3])}, rw, lane * 16, WS_GU + rho * 1024, AUX_SC1);
      if (hs) {
        WAIT_GO();
        v4u pv[3];
#pragma unroll 1
        for (unsigned spin = 0; spin < SPIN_CAP; ++spin) {
          POLL_FENCE();
          bool ok = true;
#pragma unroll
          for (int k = 0; k < 3; ++k) pv[k] = __builtin_amdgcn_raw_buffer_load_b128(rw, lane * 16, WS_GU + (rho + 1 + k) * 1024, AUX_SC1);
#pragma unroll
          for (int k = 0; k < 3; ++k) ok = ok && pv[k][0] != SENT && pv[k][1] != SENT && pv[k][2] != SENT && pv[k][3] != SENT;
          if (__all(ok)) break;
          __builtin_amdgcn_s_sleep(32);
        }
        float hh[4];
#pragma unroll
        for (int k = 0; k < 4; ++k) hh[k] = rbf(sigmoidf_(rbf(g4[k] + __uint_as_float(pv[0][k])))) * rbf(__uint_as_float(pv[1][k]) + __uint_as_float(pv[2][k]));
        const bool live = (rho >> 2) * 64 + 4 * c < FF;
#pragma unroll
        for (int k = 0; k < 2; ++k) {
          unsigned hp = live ? pk_bf16(hh[2 * k], hh[2 * k + 1]) : 0u;
          if (hp == SENT) hp = 0x7FC07FC0u;
          __builtin_amdgcn_raw_buffer_store_b32(hp, rw, (2 * c + k) * 16 + r * 4, WS_H + (rho >> 2) * 512, AUX_SC1);
        }
      }
    }
    lds_wait(&lds[L_SC + 2], NW);
    const float mw = lds[L_M + (lane & 7)];
    float M = fmaxf(mw, dpp<XOR1>(mw)); M = fmaxf(M, dpp<XOR2>(M)); M = fmaxf(M, dpp<XOR7>(M));
    const float ew = __builtin_amdgcn_exp2f(mw - M);
    float L = ew * lds[L_L + (lane & 7)]; L += dpp<XOR1>(L); L += dpp<XOR2>(L); L += dpp<XOR7>(L);
    if (lane == 0) __hip_atomic_store(ML + s, ((unsigned long long)canon(L) << 32) | canon(M), RLX_AGENT);
    if (!dn && !hs) WAIT_GO();
    lds_wait(&lds[L_SC + 3], 6);
    unsigned long long pm = ~0ull;
#pragma unroll 1
    for (unsigned spin = 0; spin < SPIN_CAP; ++spin) {
      POLL_FENCE();
      pm = __hip_atomic_load(ML + (s ^ 1), RLX_AGENT);
      if (__builtin_amdgcn_readfirstlane((unsigned)pm) != SENT) break;
      __builtin_amdgcn_s_sleep(32);
    }
    {
      const float M2 = __uint_as_float((unsigned)pm), L2 = __uint_as_float((unsigned)(pm >> 32));
      const float Mx = fmaxf(M, M2), e1 = __builtin_amdgcn_exp2f(M - Mx), e2 = __builtin_amdgcn_exp2f(M2 - Mx);
      if (lane < 8) lds[L_F + lane] = ew * e1 / (e1 * L + e2 * L2);
    }
    const int u = chunk & 63;
    float mix = 0.f;
#pragma unroll 1
    for (int round = 0; round < 2; ++round) {
      const int yo = round ? WS_Y + (b * 64) * 2048 + u * 32 : WS_GU + (NGU + (u >> 3)) * 1024 + b * 256 + (u & 7) * 32;
      const int vo = round ? lane * 2048 : lane * 8192;
      v4u y0 = {0u, 0u, 0u, 0u}, y1 = y0;
      bool ok = !round && lane >= NDN / 8;
      BAR();
#pragma unroll 1
      for (unsigned spin = 0; spin < SPIN_CAP; ++spin) {
        POLL_FENCE();
        if (!ok) {
          y0 = __builtin_amdgcn_raw_buffer_load_b128(rw, vo, yo, AUX_SC1);
          y1 = __builtin_amdgcn_raw_buffer_load_b128(rw, vo, yo + 16, AUX_SC1);
          ok = y0[0] != SENT && y0[1] != SENT && y0[2] != SENT && y0[3] != SENT && y1[0] != SENT && y1[1] != SENT && y1[2] != SENT && y1[3] != SENT;
        }
        if (__all(ok)) break;
        __builtin_amdgcn_s_sleep(2);
      }
      float xs[8];
#pragma unroll
      for (int k = 0; k < 4; ++k) { xs[k] = __uint_as_float(y0[k]); xs[4 + k] = __uint_as_float(y1[k]); }
      TR_STEP(4, XOR8, 8) TR_STEP(2, XOR7, 4) TR_STEP(1, XOR2, 2)
      float o = xs[0]; o += dpp<XOR1>(o); o = xrow_sum(o);
      mix += rbf((round ? gate_b : rbf(1.0f - gate_b)) * rbf(o));
    }
    if (lane < 16 && !(lane & 1)) out[b * DM + u * 8 + (lane >> 1)] = __builtin_bit_cast(unsigned short, (__bf16)mix);
    return;
  }

  const __amdgpu_buffer_rsrc_t rk = __builtin_amdgcn_make_buffer_rsrc((void*)kc, 0, NB * HQ * SK * DH * 2, RSW3);
  const __amdgpu_buffer_rsrc_t rv = __builtin_amdgcn_make_buffer_rsrc((void*)vc, 0, NB * HQ * SK * DH * 2, RSW3);
  const __amdgpu_buffer_rsrc_t rq = __builtin_amdgcn_make_buffer_rsrc((void*)wq, 0, DM * HQ * DH * 2, RSW3);
  const __amdgpu_buffer_rsrc_t ro = __builtin_amdgcn_make_buffer_rsrc((void*)wo, 0, DM * HQ * DH * 2, RSW3);

  const int kvoff = lane * 16;
  const int kvbase = chunk * (512 * 1024) + w * 4096;
  const int r0 = chunk * 4;
#define KVOFF(t) (kvbase + ((((t) + r0) & 15) << 15))
  v4u wq_[16], wt[16];
  v4u a[16];
  unsigned xd[8];
  const int oso = (h * DH + 16 * w) * (DM * 2);
  const int qvoff = r * 2 * (HQ * DH * 2) + c * 16, qso = 8 * w * (HQ * DH * 2) + h * DH * 2;
  const int rot = 2 * b;
#define KLOAD(n) { a[n] = __builtin_amdgcn_raw_buffer_load_b128(rk, kvoff, KVOFF((n) >> 2) + ((n) & 3) * 1024, AUX_NT); SB(); }
#define LD_O(i) { wt[i] = __builtin_amdgcn_raw_buffer_load_b128(ro, kvoff, oso + (((i) + 2 * m8) & 15) * (DM * 2), 0); SB(); }
#define LD_Q(i) { wq_[i] = __builtin_amdgcn_raw_buffer_load_b128(rq, qvoff, qso + (64 * ((((i) >> 1) + rot) & 7) + ((i) & 1)) * (HQ * DH * 2), 0); SB(); }
#define LD_X() { _Pragma("unroll") for (int k = 0; k < 8; ++k) xd[k] = __builtin_amdgcn_raw_buffer_load_b32(rx, r * 4, (b * DM + 64 * ((k + rot) & 7) + 8 * w) * 2, 0); SB(); }
  const bool early = s == 0;
  if (early) {
#pragma unroll
    for (int i = 0; i < 12; ++i) LD_Q(i)
    LD_X()
#pragma unroll
    for (int n = 0; n < K0; ++n) KLOAD(n)
#pragma unroll
    for (int i = 12; i < 16; ++i) LD_Q(i)
  } else {
#pragma unroll
    for (int n = 0; n < 16; ++n) KLOAD(n)
  }
  BAR();
  SB();
  if (!early) {
    LD_X()
#pragma unroll
    for (int i = 0; i < 16; ++i) LD_Q(i)
  }
  {
    float qa[8];
#pragma unroll
    for (int k = 0; k < 8; ++k) qa[k] = 0.f;
#pragma unroll
    for (int i = 0; i < 16; ++i) {
      const unsigned xx = xd[i >> 1];
      const unsigned xl = (i & 1) ? (xx >> 16) : (xx & 0xffffu), xh = (i & 1) ? (xx & 0xffff0000u) : (xx << 16);
#pragma unroll
      for (int k = 0; k < 4; ++k) { qa[2 * k] = dot2(wq_[i][k], xl, qa[2 * k]); qa[2 * k + 1] = dot2(wq_[i][k], xh, qa[2 * k + 1]); }
    }
#pragma unroll
    for (int k = 0; k < 8; ++k) qa[k] = xrow_sum(qa[k]);
    KEEP8(qa);
    if (r == 0) {
      *(f32x4*)&lds[L_QP + w * 128 + c * 8] = f32x4{qa[0], qa[1], qa[2], qa[3]};
      *(f32x4*)&lds[L_QP + w * 128 + c * 8 + 4] = f32x4{qa[4], qa[5], qa[6], qa[7]};
    }
    SB();
  }
  if (early) {
#pragma unroll
    for (int n = K0; n < 16; ++n) KLOAD(n)
  }
  BAR();
  unsigned qpk[4];
  {
    f32x4 q0 = {0.f, 0.f, 0.f, 0.f}, q1 = q0;
#pragma unroll
    for (int ww = 0; ww < NW; ++ww) { q0 += *(const f32x4*)&lds[L_QP + ww * 128 + c * 8]; q1 += *(const f32x4*)&lds[L_QP + ww * 128 + c * 8 + 4]; }
    qpk[0] = pk_bf16(q0[0], q0[1]); qpk[1] = pk_bf16(q0[2], q0[3]); qpk[2] = pk_bf16(q1[0], q1[1]); qpk[3] = pk_bf16(q1[2], q1[3]);
    asm volatile("" : "+v"(qpk[0]), "+v"(qpk[1]), "+v"(qpk[2]), "+v"(qpk[3]));
    SB();
  }
  float* prow = &lds[L_P + (w * 4 + r) * 64];
  float mx = -INFINITY;
#define PACE_IN(st) { if ((st) >= 1) lds_wait(&lds[L_SC + 1], NW * (st)); }
#define PACE_OUT() { if (lane == 0) __hip_atomic_fetch_add((lu32*)&lds[L_SC + 1], 1u, RLX_WG); }
#define KSTEP(T, RN) { \
    PACE_IN(T) \
    float xs[16]; \
    _Pragma("unroll") for (int g = 0; g < 4; ++g) { \
      const int o = KVOFF(4 * (T) + g + 4); \
      _Pragma("unroll") for (int i = 0; i < 4; ++i) { \
        v4u& kk = a[g * 4 + i]; \
        float d = dot2(kk[0], qpk[0], 0.f); d = dot2(kk[1], qpk[1], d); d = dot2(kk[2], qpk[2], d); d = dot2(kk[3], qpk[3], d); \
        asm volatile("" : "+v"(d)); \
        xs[g * 4 + i] = d; \
        SB(); \
        kk = __builtin_amdgcn_raw_buffer_load_b128(RN, kvoff, o + i * 1024, AUX_NT); \
        SB(); \
      } \
      LD_O(4 * (T) + g) \
    } \
    PACE_OUT() \
    TR_STEP(8, XOR8, 8) TR_STEP(4, XOR7, 4) TR_STEP(2, XOR2, 2) TR_STEP(1, XOR1, 1) \
    const float sc = rbf(rbf(xs[0]) * SCALE) * LOG2E; \
    prow[16 * (T) + c] = sc; \
    mx = fmaxf(mx, sc); \
    if (tid == 64 * (NW - 1)) __hip_atomic_store((lu32*)&lds[L_SC + 3], (unsigned)(T) + 1u, RLX_WG); }
  KSTEP(0, rk) KSTEP(1, rk) KSTEP(2, rk) KSTEP(3, rv)
  mx = xrow_max(row_max(mx));
  {
    float ls = 0.f;
#pragma unroll
    for (int T = 0; T < 4; ++T) { const float p = __builtin_amdgcn_exp2f(prow[16 * T + c] - mx); prow[16 * T + c] = p; ls += p; }
    ls = xrow_sum(row_sum(ls));
    if (lane == 0) { lds[L_M + w] = mx; lds[L_L + w] = ls; lds_arrive(&lds[L_SC + 2]); }
  }
  LDS_DONE();
  SB();
#define WPERM(p) { _Pragma("unroll") for (int k = 0; k < 4; ++k) { const unsigned lo_ = __builtin_amdgcn_perm(wt[2 * (p) + 1][k], wt[2 * (p)][k], 0x05040100u), \
      hi_ = __builtin_amdgcn_perm(wt[2 * (p) + 1][k], wt[2 * (p)][k], 0x07060302u); wt[2 * (p)][k] = lo_; wt[2 * (p) + 1][k] = hi_; } \
      asm volatile("" : "+v"(wt[2 * (p)]), "+v"(wt[2 * (p) + 1])); }
  WPERM(0) WPERM(1) WPERM(2) WPERM(3) WPERM(4) WPERM(5)
  SB();
  float oa[8];
#pragma unroll
  for (int k = 0; k < 8; ++k) oa[k] = 0.f;
#pragma unroll 1
  for (int T = 0; T < 4; ++T) {
    PACE_IN(4 + T)
#pragma unroll
    for (int g = 0; g < 4; ++g) {
      const int un = 4 * T + g + 4;
      const int o = un < 16 ? KVOFF(un) : OOB;
      const f32x4 p4 = *(const f32x4*)&prow[16 * T + 4 * g];
#pragma unroll
      for (int i = 0; i < 4; ++i) {
        v4u& vv = a[g * 4 + i];
#pragma unroll
        for (int k = 0; k < 4; ++k) { oa[2 * k] = fmaf(p4[i], bflo(vv[k]), oa[2 * k]); oa[2 * k + 1] = fmaf(p4[i], bfhi(vv[k]), oa[2 * k + 1]); }
        KEEP8(oa);
        SB();
        vv = __builtin_amdgcn_raw_buffer_load_b128(rv, kvoff, o + i * 1024, AUX_NT);
        SB();
      }
    }
    PACE_OUT()
    if (tid == 64 * (NW - 1)) __hip_atomic_store((lu32*)&lds[L_SC + 3], (unsigned)T + 5u, RLX_WG);
  }
  WPERM(6) WPERM(7)
#pragma unroll
  for (int k = 0; k < 8; ++k) oa[k] = xrow_sum(oa[k]);
  if (r == 0) {
    *(f32x4*)&lds[L_OP + w * 128 + c * 8] = f32x4{oa[0], oa[1], oa[2], oa[3]};
    *(f32x4*)&lds[L_OP + w * 128 + c * 8 + 4] = f32x4{oa[4], oa[5], oa[6], oa[7]};
  }
  BAR();
  float od = 0.f;
#pragma unroll
  for (int ww = 0; ww < NW; ++ww) od = fmaf(lds[L_F + ww], lds[L_OP + ww * 128 + 16 * w + ((c + 2 * m8) & 15)], od);
  const unsigned opk = pk_bf16(od, dpp<XOR1>(od));
  float ya[8];
#pragma unroll
  for (int k = 0; k < 8; ++k) ya[k] = 0.f;
#pragma unroll
  for (int p = 0; p < 8; ++p) {
    const unsigned ov = __builtin_amdgcn_readlane(opk, 2 * p);
#pragma unroll
    for (int k = 0; k < 4; ++k) { ya[2 * k] = dot2(wt[2 * p][k], ov, ya[2 * k]); ya[2 * k + 1] = dot2(wt[2 * p + 1][k], ov, ya[2 * k + 1]); }
  }
  *(f32x4*)&lds[L_YP + w * 512 + lane * 8] = f32x4{ya[0], ya[1], ya[2], ya[3]};
  *(f32x4*)&lds[L_YP + w * 512 + lane * 8 + 4] = f32x4{ya[4], ya[5], ya[6], ya[7]};
  BAR();
  if (w < 2) {
    f32x4 y = *(const f32x4*)&lds[L_YP + tid * 4];
#pragma unroll
    for (int ww = 1; ww < NW; ++ww) y += *(const f32x4*)&lds[L_YP + ww * 512 + tid * 4];
    const v4u yu = {canon(y[0]), canon(y[1]), canon(y[2]), canon(y[3])};
    __builtin_amdgcn_raw_buffer_store_b128(yu, rw, tid * 16, WS_Y + chunk * 2048, AUX_SC1);
  }
}
}

extern "C" void kernel_launch(void* const* d_in, const int* in_sizes, int n_in, void* d_out, int out_size, void* d_ws, size_t ws_size,
                              hipStream_t stream) {
  typedef const unsigned short* P;
  hipLaunchKernelGGL(k_main, dim3(NWG), dim3(NTHR), 0, stream, (P)d_in[1], (P)d_in[3], (P)d_in[0], (P)d_in[2], (P)d_in[4], (unsigned char*)d_ws,
                     (P)d_in[9], (unsigned short*)d_out, (P)d_in[6], (P)d_in[7], (P)d_in[8]);
}
```

```cpp
#include <hip/hip_runtime.h>
#include <hip/hip_bf16.h>
#include <cstdint>

namespace {
typedef unsigned v4u __attribute__((ext_vector_type(4)));
typedef unsigned v2u __attribute__((ext_vector_type(2)));
typedef float f32x4 __attribute__((ext_vector_type(4)));
typedef __bf16 bf16x2 __attribute__((ext_vector_type(2)));
#define GAS __attribute__((address_space(1)))
typedef GAS unsigned gu32;
typedef GAS unsigned long long gu64;
typedef __attribute__((address_space(3))) unsigned lu32;
#define RLX_AGENT __ATOMIC_RELAXED, __HIP_MEMORY_SCOPE_AGENT
#define RLX_WG __ATOMIC_RELAXED, __HIP_MEMORY_SCOPE_WORKGROUP

constexpr int NB = 4, DM = 512, HQ = 32, SK = 4096, DH = 128, FF = 1376;
constexpr int NWG = 256, NW = 8, NTHR = 64 * (NW + 1);
constexpr int NDN = 48;
constexpr int NGU = 88;
constexpr int RSW3 = 0x00020000;
constexpr int AUX_NT = 2, AUX_SC1 = 16;
constexpr int OOB = 0x40000000;
constexpr float LOG2E = 1.4426950408889634f;
constexpr float SCALE = 0.08838834764831845f;
constexpr unsigned SENT = 0xFFFFFFFFu;
constexpr unsigned SPIN_CAP = 1u << 18;
constexpr int K0 = 4;

constexpr int LS = 384;
constexpr int WS_R = 0;
constexpr int WS_E = 64 * LS;
constexpr int WS_ML = 128 * LS;
constexpr int WS_Y = (WS_ML + 128 * LS + 4095) & ~4095;
constexpr int WS_GU = WS_Y + NWG * 2048;
constexpr int WS_H = WS_GU + (NGU + NDN) * 1024;
constexpr int WS_END = WS_H + 704 * 16;

constexpr int L_QP = 0;
constexpr int L_P = L_QP + 8 * 128;
constexpr int L_M = L_P + 8 * 256;
constexpr int L_L = L_M + 8;
constexpr int L_SC = L_L + 8;
constexpr int L_F = L_SC + 8;
constexpr int L_OP = L_F + 8;
constexpr int L_YP = L_OP + 8 * 128;
constexpr int L_XT = L_YP + 8 * 512;
constexpr int L_END = L_XT + 256 * 4;

__device__ __forceinline__ float dot2(unsigned a, unsigned b, float acc) {
  return __builtin_amdgcn_fdot2_f32_bf16(__builtin_bit_cast(bf16x2, a), __builtin_bit_cast(bf16x2, b), acc, false);
}
__device__ __forceinline__ float bflo(unsigned u) { return __builtin_bit_cast(float, u << 16); }
__device__ __forceinline__ float bfhi(unsigned u) { return __builtin_bit_cast(float, u & 0xffff0000u); }
__device__ __forceinline__ unsigned pk_bf16(float lo, float hi) {
  bf16x2 r; r.x = (__bf16)lo; r.y = (__bf16)hi; return __builtin_bit_cast(unsigned, r);
}
__device__ __forceinline__ float rbf(float x) { return (float)(__bf16)x; }
template <int CTRL> __device__ __forceinline__ float dpp(float x) {
  return __builtin_bit_cast(float, __builtin_amdgcn_mov_dpp(__builtin_bit_cast(int, x), CTRL, 0xf, 0xf, true));
}
constexpr int XOR1 = 0xB1, XOR2 = 0x4E, XOR7 = 0x141, XOR8 = 0x128;
__device__ __forceinline__ float xrow_sum(float x) {
  auto s = __builtin_amdgcn_permlane16_swap(__float_as_uint(x), __float_as_uint(x), false, false);
  x = __uint_as_float(s[0]) + __uint_as_float(s[1]);
  auto t = __builtin_amdgcn_permlane32_swap(__float_as_uint(x), __float_as_uint(x), false, false);
  return __uint_as_float(t[0]) + __uint_as_float(t[1]);
}
__device__ __forceinline__ float xrow_max(float x) {
  auto s = __builtin_amdgcn_permlane16_swap(__float_as_uint(x), __float_as_uint(x), false, false);
  x = fmaxf(__uint_as_float(s[0]), __uint_as_float(s[1]));
  auto t = __builtin_amdgcn_permlane32_swap(__float_as_uint(x), __float_as_uint(x), false, false);
  return fmaxf(__uint_as_float(t[0]), __uint_as_float(t[1]));
}
__device__ __forceinline__ float row_sum(float x) { x += dpp<XOR1>(x); x += dpp<XOR2>(x); x += dpp<XOR7>(x); x += dpp<XOR8>(x); return x; }
__device__ __forceinline__ float row_max(float x) { x = fmaxf(x, dpp<XOR1>(x)); x = fmaxf(x, dpp<XOR2>(x)); x = fmaxf(x, dpp<XOR7>(x)); x = fmaxf(x, dpp<XOR8>(x)); return x; }
__device__ __forceinline__ float sigmoidf_(float x) { return 1.0f / (1.0f + __expf(-x)); }
__device__ __forceinline__ unsigned canon(float v) { const unsigned u = __float_as_uint(v); return u == SENT ? 0x7FC00000u : u; }
#define SB() __builtin_amdgcn_sched_barrier(0)
#define POLL_FENCE() asm volatile("" ::: "memory")
#define DRAIN() asm volatile("s_waitcnt vmcnt(0)" ::: "memory")
#define LDS_DONE() asm volatile("s_waitcnt lgkmcnt(0)" ::: "memory")
#define BAR() do { LDS_DONE(); __builtin_amdgcn_s_barrier(); asm volatile("" ::: "memory"); } while (0)
#define KEEP8(a) asm volatile("" : "+v"(a[0]), "+v"(a[1]), "+v"(a[2]), "+v"(a[3]), "+v"(a[4]), "+v"(a[5]), "+v"(a[6]), "+v"(a[7]))
#define TR_STEP(HALF, CTRL, BIT) _Pragma("unroll") for (int hh = 0; hh < (HALF); ++hh) { \
      const float lo_ = xs[hh], hi_ = xs[hh + (HALF)]; const float t1_ = lo_ + dpp<CTRL>(lo_), t2_ = hi_ + dpp<CTRL>(hi_); \
      xs[hh] = (c & (BIT)) ? t2_ : t1_; }

__device__ __forceinline__ void lds_arrive(float* p) { LDS_DONE(); __hip_atomic_fetch_add((lu32*)p, 1u, RLX_WG); }
__device__ __forceinline__ void lds_wait(float* p, unsigned n) {
#pragma unroll 1
  for (unsigned spin = 0; spin < (SPIN_CAP << 4); ++spin) {
    if (__builtin_amdgcn_readfirstlane(__hip_atomic_load((lu32*)p, RLX_WG)) >= n) break;
    __builtin_amdgcn_s_sleep(1);
  }
  asm volatile("" ::: "memory");
}

__global__ void __launch_bounds__(NTHR, 3)
k_main(const unsigned short* __restrict__ kc, const unsigned short* __restrict__ wq, const unsigned short* __restrict__ x,
       const unsigned short* __restrict__ vc, const unsigned short* __restrict__ wo, unsigned char* __restrict__ ws,
       const unsigned short* __restrict__ gw, unsigned short* __restrict__ out,
       const unsigned short* __restrict__ wg, const unsigned short* __restrict__ wu, const unsigned short* __restrict__ wd) {
  __shared__ __attribute__((aligned(16))) float lds[L_END];
  asm volatile("" :: "s"(x), "s"(kc), "s"(vc), "s"(wq), "s"(wo), "s"(ws), "s"(gw), "s"(out), "s"(wg), "s"(wu), "s"(wd));
  const int tid = threadIdx.x, lane = tid & 63, w = __builtin_amdgcn_readfirstlane(tid >> 6), r = lane >> 4, c = lane & 15;
  const int bid = blockIdx.x, xg = bid & 7, j = bid >> 3;
  const int h = xg * 4 + (j >> 3), b = (j >> 1) & 3, s = j & 1, m8 = j & 7;
  const int chunk = (b * HQ + h) * 2 + s;
  const __amdgpu_buffer_rsrc_t rx = __builtin_amdgcn_make_buffer_rsrc((void*)x, 0, NB * DM * 2, RSW3);
  const __amdgpu_buffer_rsrc_t rw = __builtin_amdgcn_make_buffer_rsrc((void*)ws, 0, WS_END, RSW3);

  if (w == NW) {
    gu32* R = (gu32*)(ws + WS_R + (chunk >> 2) * LS) + (chunk & 3);
    gu32* E = (gu32*)(ws + WS_E + (chunk >> 2) * LS) + (chunk & 3);
    gu64* ML = (gu64*)(ws + WS_ML + (chunk >> 1) * LS);
    __builtin_amdgcn_s_setprio(3);
    if (lane < 3) ((unsigned*)lds)[L_SC + 1 + lane] = 0u;
    const int rho = (bid & 1) ? (bid < 18 ? 127 + (bid >> 1) : 999) : (bid >> 1) - 1;
    const bool gu = (unsigned)rho < (unsigned)NGU, dn = rho >= NGU && rho < NGU + NDN, hs = gu && !(rho & 3);
    const int dd = rho - NGU;
    const unsigned r_old = __hip_atomic_load(R, RLX_AGENT), e_old = __hip_atomic_load(E, RLX_AGENT);
    const v4u rfirst = __builtin_amdgcn_raw_buffer_load_b128(rw, lane * LS, bid == 0 ? WS_R : OOB, AUX_SC1);
    v4u xv[4];
#pragma unroll
    for (int bb = 0; bb < 4; ++bb) xv[bb] = __builtin_amdgcn_raw_buffer_load_b128(rx, lane * 16, bb * DM * 2, 0);
    const v4u gv = *(const GAS v4u*)((const GAS unsigned char*)gw + lane * 16);
    const v4u sent4 = {SENT, SENT, SENT, SENT};
    __builtin_amdgcn_raw_buffer_store_b128(sent4, rw, lane * 16, WS_Y + chunk * 2048, AUX_SC1);
    __builtin_amdgcn_raw_buffer_store_b128(sent4, rw, lane * 16, WS_Y + chunk * 2048 + 1024, AUX_SC1);
    if (gu || dn) __builtin_amdgcn_raw_buffer_store_b128(sent4, rw, lane * 16, WS_GU + rho * 1024, AUX_SC1);
    if (hs && lane < 32) __builtin_amdgcn_raw_buffer_store_b128(sent4, rw, lane * 16, WS_H + (rho >> 2) * 512, AUX_SC1);
    if (lane == 0) __hip_atomic_store(ML + s, ~0ull, RLX_AGENT);
    BAR();
    __builtin_amdgcn_s_setprio(0);
    const __amdgpu_buffer_rsrc_t rm = __builtin_amdgcn_make_buffer_rsrc((void*)(dn ? wd : (rho & 2) ? wu : wg), 0, (gu || dn) ? DM * FF * 2 : 0, RSW3);
    const int rowb = dn ? DM * 2 : FF * 2;
    const int mvo = (lane >> 3) * 2 * rowb + (lane & 7) * 16;
    const int mso = (dn ? dd >> 3 : rho & 1) * 256 * rowb + (dn ? dd & 7 : rho >> 2) * 128, istep = 16 * rowb;
    v4u m0[8], m1[8];
    if (gu || dn) {
#pragma unroll
      for (int i = 0; i < 8; ++i) {
        m0[i] = __builtin_amdgcn_raw_buffer_load_b128(rm, mvo, mso + i * istep, AUX_NT); SB();
        m1[i] = __builtin_amdgcn_raw_buffer_load_b128(rm, mvo, mso + i * istep + rowb, AUX_NT); SB();
      }
    }
    __builtin_amdgcn_s_setprio(3);
    BAR();
    unsigned nonce = (r_old & ~3u) + 4u; if (e_old - nonce < 4u) nonce += 4u;
    float gate_b;
    {
      const v4u xb = b == 0 ? xv[0] : b == 1 ? xv[1] : b == 2 ? xv[2] : xv[3];
      float gd = 0.f;
#pragma unroll
      for (int k = 0; k < 4; ++k) gd += rbf(bflo(xb[k]) * bflo(gv[k])) + rbf(bfhi(xb[k]) * bfhi(gv[k]));
      gate_b = rbf(sigmoidf_(rbf(xrow_sum(row_sum(gd)))));
    }
#pragma unroll
    for (int k = 0; k < 4; ++k) *(v4u*)&lds[L_XT + (4 * lane + k) * 4] = v4u{xv[0][k], xv[1][k], xv[2][k], xv[3][k]};
    DRAIN();
    if (lane == 0) __hip_atomic_store(R, nonce, RLX_AGENT);
    if (bid == 0) {
      unsigned seen = 0;
#pragma unroll 1
      for (unsigned spin = 0; spin < SPIN_CAP; ++spin) {
        POLL_FENCE();
        __builtin_amdgcn_s_sleep(40);
        const v4u rq = __builtin_amdgcn_raw_buffer_load_b128(rw, lane * LS, WS_R, AUX_SC1);
#pragma unroll
        for (int k = 0; k < 4; ++k) seen |= (rq[k] != rfirst[k]) ? 1u << k : 0u;
        if (__all(seen == 15u)) {
          __builtin_amdgcn_raw_buffer_store_b128((rq & ~3u) + 2u, rw, lane * LS, WS_E, AUX_SC1);
          break;
        }
        if (spin >= 2) {
#pragma unroll
          for (int k = 0; k < 4; ++k) if (!(seen >> k & 1u)) __hip_atomic_store((gu32*)(ws + WS_E + lane * LS) + k, rq[k], RLX_AGENT);
        }
      }
    }
#define WAIT_GO() do { _Pragma("unroll 1") for (unsigned spin = 0; spin < SPIN_CAP; ++spin) { POLL_FENCE(); \
        const unsigned e_ = __builtin_amdgcn_readfirstlane(__hip_atomic_load(E, RLX_AGENT)); \
        if (e_ == nonce + 2u) break; \
        if (e_ == nonce && lane == 0) __hip_atomic_store(R, nonce + 1u, RLX_AGENT); \
        __builtin_amdgcn_s_sleep(32); } } while (0)
    if (gu || dn) {
      if (dn) {
        lds_wait(&lds[L_SC + 3], 3);
        WAIT_GO();
        v4u hv[2];
        const int ho = WS_H + (dd >> 3) * 2048;
#pragma unroll 1
        for (unsigned spin = 0; spin < SPIN_CAP; ++spin) {
          POLL_FENCE();
          bool ok = true;
#pragma unroll
          for (int k = 0; k < 2; ++k) hv[k] = __builtin_amdgcn_raw_buffer_load_b128(rw, lane * 16, ho + k * 1024, AUX_SC1);
#pragma unroll
          for (int k = 0; k < 2; ++k) ok = ok && hv[k][0] != SENT && hv[k][1] != SENT && hv[k][2] != SENT && hv[k][3] != SENT;
          if (__all(ok)) break;
          __builtin_amdgcn_s_sleep(32);
        }
        *(v4u*)&lds[L_XT + lane * 4] = hv[0];
        *(v4u*)&lds[L_XT + 256 + lane * 4] = hv[1];
        LDS_DONE();
      }
      const float* xt = &lds[L_XT + ((dn ? 0 : rho & 1) * 128 + (lane >> 3)) * 4];
      constexpr int xstep = 32;
      float acc[4][8];
#pragma unroll
      for (int bb = 0; bb < 4; ++bb)
#pragma unroll
        for (int k = 0; k < 8; ++k) acc[bb][k] = 0.f;
#pragma unroll 1
      for (int I0 = 0; I0 < 16; I0 += 8) {
#pragma unroll
        for (int i = 0; i < 8; ++i) {
          __builtin_amdgcn_s_setprio(0);
          const v4u xp = *(const v4u*)(xt + (I0 + i) * xstep);
#pragma unroll
          for (int k = 0; k < 4; ++k) {
            const unsigned lo = __builtin_amdgcn_perm(m1[i][k], m0[i][k], 0x05040100u), hi = __builtin_amdgcn_perm(m1[i][k], m0[i][k], 0x07060302u);
#pragma unroll
            for (int bb = 0; bb < 4; ++bb) { acc[bb][2 * k] = dot2(lo, xp[bb], acc[bb][2 * k]); acc[bb][2 * k + 1] = dot2(hi, xp[bb], acc[bb][2 * k + 1]); }
          }
          KEEP8(acc[0]); KEEP8(acc[1]); KEEP8(acc[2]); KEEP8(acc[3]);
          SB();
          __builtin_amdgcn_s_setprio(3);
          const int o = I0 == 0 ? mso + (8 + i) * istep : OOB;
          m0[i] = __builtin_amdgcn_raw_buffer_load_b128(rm, mvo, o, AUX_NT); SB();
          m1[i] = __builtin_amdgcn_raw_buffer_load_b128(rm, mvo, o + rowb, AUX_NT); SB();
        }
      }
#pragma unroll
      for (int bb = 0; bb < 4; ++bb) {
        *(f32x4*)&lds[L_YP + lane * 32 + bb * 8] = f32x4{acc[bb][0], acc[bb][1], acc[bb][2], acc[bb][3]};
        *(f32x4*)&lds[L_YP + lane * 32 + bb * 8 + 4] = f32x4{acc[bb][4], acc[bb][5], acc[bb][6], acc[bb][7]};
      }
      LDS_DONE();
      f32x4 g4 = {0.f, 0.f, 0.f, 0.f};
      const float* sp = &lds[L_YP + (c >> 1) * 32 + r * 8 + (c & 1) * 4];
#pragma unroll
      for (int rg = 0; rg < 8; ++rg) g4 += *(const f32x4*)(sp + rg * 256);
      __builtin_amdgcn_raw_buffer_store_b128(v4u{canon(g4[0]), canon(g4[1]), canon(g4[2]), canon(g4[3])}, rw, lane * 16, WS_GU + rho * 1024, AUX_SC1);
      if (hs) {
        WAIT_GO();
        v4u pv[3];
#pragma unroll 1
        for (unsigned spin = 0; spin < SPIN_CAP; ++spin) {
          POLL_FENCE();
          bool ok = true;
#pragma unroll
          for (int k = 0; k < 3; ++k) pv[k] = __builtin_amdgcn_raw_buffer_load_b128(rw, lane * 16, WS_GU + (rho + 1 + k) * 1024, AUX_SC1);
#pragma unroll
          for (int k = 0; k < 3; ++k) ok = ok && pv[k][0] != SENT && pv[k][1] != SENT && pv[k][2] != SENT && pv[k][3] != SENT;
          if (__all(ok)) break;
          __builtin_amdgcn_s_sleep(32);
        }
        float hh[4];
#pragma unroll
        for (int k = 0; k < 4; ++k) hh[k] = rbf(sigmoidf_(rbf(g4[k] + __uint_as_float(pv[0][k])))) * rbf(__uint_as_float(pv[1][k]) + __uint_as_float(pv[2][k]));
        const bool live = (rho >> 2) * 64 + 4 * c < FF;
#pragma unroll
        for (int k = 0; k < 2; ++k) {
          unsigned hp = live ? pk_bf16(hh[2 * k], hh[2 * k + 1]) : 0u;
          if (hp == SENT) hp = 0x7FC07FC0u;
          __builtin_amdgcn_raw_buffer_store_b32(hp, rw, (2 * c + k) * 16 + r * 4, WS_H + (rho >> 2) * 512, AUX_SC1);
        }
      }
    }
    lds_wait(&lds[L_SC + 2], NW);
    const float mw = lds[L_M + (lane & 7)];
    float M = fmaxf(mw, dpp<XOR1>(mw)); M = fmaxf(M, dpp<XOR2>(M)); M = fmaxf(M, dpp<XOR7>(M));
    const float ew = __builtin_amdgcn_exp2f(mw - M);
    float L = ew * lds[L_L + (lane & 7)]; L += dpp<XOR1>(L); L += dpp<XOR2>(L); L += dpp<XOR7>(L);
    if (lane == 0) __hip_atomic_store(ML + s, ((unsigned long long)canon(L) << 32) | canon(M), RLX_AGENT);
    if (!dn && !hs) WAIT_GO();
    lds_wait(&lds[L_SC + 3], 6);
    unsigned long long pm = ~0ull;
#pragma unroll 1
    for (unsigned spin = 0; spin < SPIN_CAP; ++spin) {
      POLL_FENCE();
      pm = __hip_atomic_load(ML + (s ^ 1), RLX_AGENT);
      if (__builtin_amdgcn_readfirstlane((unsigned)pm) != SENT) break;
      __builtin_amdgcn_s_sleep(32);
    }
    {
      const float M2 = __uint_as_float((unsigned)pm), L2 = __uint_as_float((unsigned)(pm >> 32));
      const float Mx = fmaxf(M, M2), e1 = __builtin_amdgcn_exp2f(M - Mx), e2 = __builtin_amdgcn_exp2f(M2 - Mx);
      if (lane < 8) lds[L_F + lane] = ew * e1 / (e1 * L + e2 * L2);
    }
    const int u = chunk & 63;
    float mix = 0.f;
#pragma unroll 1
    for (int round = 0; round < 2; ++round) {
      const int yo = round ? WS_Y + (b * 64) * 2048 + u * 32 : WS_GU + (NGU + (u >> 3)) * 1024 + b * 256 + (u & 7) * 32;
      const int vo = round ? lane * 2048 : lane * 8192;
      v4u y0 = {0u, 0u, 0u, 0u}, y1 = y0;
      bool ok = !round && lane >= NDN / 8;
      BAR();
#pragma unroll 1
      for (unsigned spin = 0; spin < SPIN_CAP; ++spin) {
        POLL_FENCE();
        if (!ok) {
          y0 = __builtin_amdgcn_raw_buffer_load_b128(rw, vo, yo, AUX_SC1);
          y1 = __builtin_amdgcn_raw_buffer_load_b128(rw, vo, yo + 16, AUX_SC1);
          ok = y0[0] != SENT && y0[1] != SENT && y0[2] != SENT && y0[3] != SENT && y1[0] != SENT && y1[1] != SENT && y1[2] != SENT && y1[3] != SENT;
        }
        if (__all(ok)) break;
        __builtin_amdgcn_s_sleep(2);
      }
      float xs[8];
#pragma unroll
      for (int k = 0; k < 4; ++k) { xs[k] = __uint_as_float(y0[k]); xs[4 + k] = __uint_as_float(y1[k]); }
      TR_STEP(4, XOR8, 8) TR_STEP(2, XOR7, 4) TR_STEP(1, XOR2, 2)
      float o = xs[0]; o += dpp<XOR1>(o); o = xrow_sum(o);
      mix += rbf((round ? gate_b : rbf(1.0f - gate_b)) * rbf(o));
    }
    if (lane < 16 && !(lane & 1)) out[b * DM + u * 8 + (lane >> 1)] = __builtin_bit_cast(unsigned short, (__bf16)mix);
    return;
  }

  const __amdgpu_buffer_rsrc_t rk = __builtin_amdgcn_make_buffer_rsrc((void*)kc, 0, NB * HQ * SK * DH * 2, RSW3);
  const __amdgpu_buffer_rsrc_t rv = __builtin_amdgcn_make_buffer_rsrc((void*)vc, 0, NB * HQ * SK * DH * 2, RSW3);
  const __amdgpu_buffer_rsrc_t rq = __builtin_amdgcn_make_buffer_rsrc((void*)wq, 0, DM * HQ * DH * 2, RSW3);
  const __amdgpu_buffer_rsrc_t ro = __builtin_amdgcn_make_buffer_rsrc((void*)wo, 0, DM * HQ * DH * 2, RSW3);

  const int kvoff = lane * 16;
  const int kvbase = chunk * (512 * 1024) + w * 4096;
  const int r0 = chunk * 4;
#define KVOFF(t) (kvbase + ((((t) + r0) & 15) << 15))
  v4u wq_[16], wt[16];
  v4u a[16];
  unsigned xd[8];
  const int oso = (h * DH + 16 * w) * (DM * 2);
  const int qvoff = r * 2 * (HQ * DH * 2) + c * 16, qso = 8 * w * (HQ * DH * 2) + h * DH * 2;
  const int rot = 2 * b;
#define KLOAD(n) { a[n] = __builtin_amdgcn_raw_buffer_load_b128(rk, kvoff, KVOFF((n) >> 2) + ((n) & 3) * 1024, AUX_NT); SB(); }
#define LD_O(i) { wt[i] = __builtin_amdgcn_raw_buffer_load_b128(ro, kvoff, oso + (((i) + 2 * m8) & 15) * (DM * 2), 0); SB(); }
#define LD_Q(i) { wq_[i] = __builtin_amdgcn_raw_buffer_load_b128(rq, qvoff, qso + (64 * ((((i) >> 1) + rot) & 7) + ((i) & 1)) * (HQ * DH * 2), 0); SB(); }
#define LD_X() { _Pragma("unroll") for (int k = 0; k < 8; ++k) xd[k] = __builtin_amdgcn_raw_buffer_load_b32(rx, r * 4, (b * DM + 64 * ((k + rot) & 7) + 8 * w) * 2, 0); SB(); }
  const bool early = s == 0;
  if (early) {
#pragma unroll
    for (int i = 0; i < 12; ++i) LD_Q(i)
    LD_X()
#pragma unroll
    for (int n = 0; n < K0; ++n) KLOAD(n)
#pragma unroll
    for (int i = 12; i < 16; ++i) LD_Q(i)
  } else {
#pragma unroll
    for (int n = 0; n < 16; ++n) KLOAD(n)
  }
  BAR();
  SB();
  if (!early) {
    LD_X()
#pragma unroll
    for (int i = 0; i < 16; ++i) LD_Q(i)
  }
  {
    float qa[8];
#pragma unroll
    for (int k = 0; k < 8; ++k) qa[k] = 0.f;
#pragma unroll
    for (int i = 0; i < 16; ++i) {
      const unsigned xx = xd[i >> 1];
      const unsigned xl = (i & 1) ? (xx >> 16) : (xx & 0xffffu), xh = (i & 1) ? (xx & 0xffff0000u) : (xx << 16);
#pragma unroll
      for (int k = 0; k < 4; ++k) { qa[2 * k] = dot2(wq_[i][k], xl, qa[2 * k]); qa[2 * k + 1] = dot2(wq_[i][k], xh, qa[2 * k + 1]); }
    }
#pragma unroll
    for (int k = 0; k < 8; ++k) qa[k] = xrow_sum(qa[k]);
    KEEP8(qa);
    if (r == 0) {
      *(f32x4*)&lds[L_QP + w * 128 + c * 8] = f32x4{qa[0], qa[1], qa[2], qa[3]};
      *(f32x4*)&lds[L_QP + w * 128 + c * 8 + 4] = f32x4{qa[4], qa[5], qa[6], qa[7]};
    }
    SB();
  }
  if (early) {
#pragma unroll
    for (int n = K0; n < 16; ++n) KLOAD(n)
  }
  BAR();
  unsigned qpk[4];
  {
    f32x4 q0 = {0.f, 0.f, 0.f, 0.f}, q1 = q0;
#pragma unroll
    for (int ww = 0; ww < NW; ++ww) { q0 += *(const f32x4*)&lds[L_QP + ww * 128 + c * 8]; q1 += *(const f32x4*)&lds[L_QP + ww * 128 + c * 8 + 4]; }
    qpk[0] = pk_bf16(q0[0], q0[1]); qpk[1] = pk_bf16(q0[2], q0[3]); qpk[2] = pk_bf16(q1[0], q1[1]); qpk[3] = pk_bf16(q1[2], q1[3]);
    asm volatile("" : "+v"(qpk[0]), "+v"(qpk[1]), "+v"(qpk[2]), "+v"(qpk[3]));
    SB();
  }
  float* prow = &lds[L_P + (w * 4 + r) * 64];
  float mx = -INFINITY;
#define PACE_IN(st) { if ((st) >= 1) lds_wait(&lds[L_SC + 1], NW * (st)); }
#define PACE_OUT() { if (lane == 0) __hip_atomic_fetch_add((lu32*)&lds[L_SC + 1], 1u, RLX_WG); }
#define KSTEP(T, RN) { \
    PACE_IN(T) \
    float xs[16]; \
    _Pragma("unroll") for (int g = 0; g < 4; ++g) { \
      const int o = KVOFF(4 * (T) + g + 4); \
      _Pragma("unroll") for (int i = 0; i < 4; ++i) { \
        v4u& kk = a[g * 4 + i]; \
        float d = dot2(kk[0], qpk[0], 0.f); d = dot2(kk[1], qpk[1], d); d = dot2(kk[2], qpk[2], d); d = dot2(kk[3], qpk[3], d); \
        asm volatile("" : "+v"(d)); \
        xs[g * 4 + i] = d; \
        SB(); \
        kk = __builtin_amdgcn_raw_buffer_load_b128(RN, kvoff, o + i * 1024, AUX_NT); \
        SB(); \
      } \
      LD_O(4 * (T) + g) \
    } \
    PACE_OUT() \
    TR_STEP(8, XOR8, 8) TR_STEP(4, XOR7, 4) TR_STEP(2, XOR2, 2) TR_STEP(1, XOR1, 1) \
    const float sc = rbf(rbf(xs[0]) * SCALE) * LOG2E; \
    prow[16 * (T) + c] = sc; \
    mx = fmaxf(mx, sc); \
    if (tid == 64 * (NW - 1)) __hip_atomic_store((lu32*)&lds[L_SC + 3], (unsigned)(T) + 1u, RLX_WG); }
  KSTEP(0, rk) KSTEP(1, rk) KSTEP(2, rk) KSTEP(3, rv)
  mx = xrow_max(row_max(mx));
  {
    float ls = 0.f;
#pragma unroll
    for (int T = 0; T < 4; ++T) { const float p = __builtin_amdgcn_exp2f(prow[16 * T + c] - mx); prow[16 * T + c] = p; ls += p; }
    ls = xrow_sum(row_sum(ls));
    if (lane == 0) { lds[L_M + w] = mx; lds[L_L + w] = ls; lds_arrive(&lds[L_SC + 2]); }
  }
  LDS_DONE();
  SB();
#define WPERM(p) { _Pragma("unroll") for (int k = 0; k < 4; ++k) { const unsigned lo_ = __builtin_amdgcn_perm(wt[2 * (p) + 1][k], wt[2 * (p)][k], 0x05040100u), \
      hi_ = __builtin_amdgcn_perm(wt[2 * (p) + 1][k], wt[2 * (p)][k], 0x07060302u); wt[2 * (p)][k] = lo_; wt[2 * (p) + 1][k] = hi_; } \
      asm volatile("" : "+v"(wt[2 * (p)]), "+v"(wt[2 * (p) + 1])); }
  WPERM(0) WPERM(1) WPERM(2) WPERM(3) WPERM(4) WPERM(5)
  SB();
  float oa[8];
#pragma unroll
  for (int k = 0; k < 8; ++k) oa[k] = 0.f;
#pragma unroll 1
  for (int T = 0; T < 4; ++T) {
    PACE_IN(4 + T)
#pragma unroll
    for (int g = 0; g < 4; ++g) {
      const int un = 4 * T + g + 4;
      const int o = un < 16 ? KVOFF(un) : OOB;
      const f32x4 p4 = *(const f32x4*)&prow[16 * T + 4 * g];
#pragma unroll
      for (int i = 0; i < 4; ++i) {
        v4u& vv = a[g * 4 + i];
#pragma unroll
        for (int k = 0; k < 4; ++k) { oa[2 * k] = fmaf(p4[i], bflo(vv[k]), oa[2 * k]); oa[2 * k + 1] = fmaf(p4[i], bfhi(vv[k]), oa[2 * k + 1]); }
        KEEP8(oa);
        SB();
        vv = __builtin_amdgcn_raw_buffer_load_b128(rv, kvoff, o + i * 1024, AUX_NT);
        SB();
      }
    }
    PACE_OUT()
    if (tid == 64 * (NW - 1)) __hip_atomic_store((lu32*)&lds[L_SC + 3], (unsigned)T + 5u, RLX_WG);
  }
  WPERM(6) WPERM(7)
#pragma unroll
  for (int k = 0; k < 8; ++k) oa[k] = xrow_sum(oa[k]);
  if (r == 0) {
    *(f32x4*)&lds[L_OP + w * 128 + c * 8] = f32x4{oa[0], oa[1], oa[2], oa[3]};
    *(f32x4*)&lds[L_OP + w * 128 + c * 8 + 4] = f32x4{oa[4], oa[5], oa[6], oa[7]};
  }
  BAR();
  float od = 0.f;
#pragma unroll
  for (int ww = 0; ww < NW; ++ww) od = fmaf(lds[L_F + ww], lds[L_OP + ww * 128 + 16 * w + ((c + 2 * m8) & 15)], od);
  const unsigned opk = pk_bf16(od, dpp<XOR1>(od));
  float ya[8];
#pragma unroll
  for (int k = 0; k < 8; ++k) ya[k] = 0.f;
#pragma unroll
  for (int p = 0; p < 8; ++p) {
    const unsigned ov = __builtin_amdgcn_readlane(opk, 2 * p);
#pragma unroll
    for (int k = 0; k < 4; ++k) { ya[2 * k] = dot2(wt[2 * p][k], ov, ya[2 * k]); ya[2 * k + 1] = dot2(wt[2 * p + 1][k], ov, ya[2 * k + 1]); }
  }
  *(f32x4*)&lds[L_YP + w * 512 + lane * 8] = f32x4{ya[0], ya[1], ya[2], ya[3]};
  *(f32x4*)&lds[L_YP + w * 512 + lane * 8 + 4] = f32x4{ya[4], ya[5], ya[6], ya[7]};
  BAR();
  if (w < 2) {
    f32x4 y = *(const f32x4*)&lds[L_YP + tid * 4];
#pragma unroll
    for (int ww = 1; ww < NW; ++ww) y += *(const f32x4*)&lds[L_YP + ww * 512 + tid * 4];
    const v4u yu = {canon(y[0]), canon(y[1]), canon(y[2]), canon(y[3])};
    __builtin_amdgcn_raw_buffer_store_b128(yu, rw, tid * 16, WS_Y + chunk * 2048, AUX_SC1);
  }
}
}

extern "C" void kernel_launch(void* const* d_in, const int* in_sizes, int n_in, void* d_out, int out_size, void* d_ws, size_t ws_size,
                              hipStream_t stream) {
  typedef const unsigned short* P;
  hipLaunchKernelGGL(k_main, dim3(NWG), dim3(NTHR), 0, stream, (P)d_in[1], (P)d_in[3], (P)d_in[0], (P)d_in[2], (P)d_in[4], (unsigned char*)d_ws,
                     (P)d_in[9], (unsigned short*)d_out, (P)d_in[6], (P)d_in[7], (P)d_in[8]);
}
```
